```python
import torch

def _softmax(x, axis=-1):
    return torch.softmax(x, dim=axis)
def _gelu(x):
    return torch.nn.functional.gelu(x, approximate="tanh")
def _silu(x):
    return torch.nn.functional.silu(x)
def _sigmoid(x):
    return torch.sigmoid(x)
def _layernorm(x, axis=-1, eps=1e-5):
    m = x.mean(dim=axis, keepdim=True)
    v = x.var(dim=axis, keepdim=True, unbiased=False)
    return (x - m) / torch.sqrt(v + eps)
def _scatter_add(tgt, idx, src, axis=0):
    return tgt.clone().index_add_(axis, idx, src)
def _segment_sum(src, seg, n):
    out = torch.zeros((n,) + src.shape[1:], dtype=src.dtype)
    return out.index_add_(0, seg, src)
def _pad(x, pw):
    flat = tuple(v for pair in reversed(pw) for v in pair)
    return torch.nn.functional.pad(x, flat)

kernel_name = 'gated_mix__mha_block__dense_activation_relu__prompt'

def setup_inputs():

    _w = (lambda a: (lambda t: t.to(torch.bfloat16) if t.is_floating_point() else t)(torch.as_tensor(a)))
    def _A_setup():
        B, S, D, H = 1, 2048, 512, 32
        return {
            "x": _w((lambda *s: torch.randn(*s, dtype=torch.bfloat16) * 0.05)(B, S, D)),
            "wqkv": _w((lambda *s: torch.randn(*s, dtype=torch.bfloat16) * 0.8)(D, 3 * D)),
            "wo": _w((lambda *s: torch.randn(*s, dtype=torch.bfloat16) * 0.2)(D, D)),
        }
    _A_in = _A_setup()

    def _B_setup():
        B, S, D, Dff = 1, 2048, 512, 1792
        return {
            "x": _w((lambda *s: torch.randn(*s, dtype=torch.bfloat16) * 0.05)(B, S, D)),
            "w1": _w((lambda *s: torch.randn(*s, dtype=torch.bfloat16) * 0.05)(D, Dff)),
            "w2": _w((lambda *s: torch.randn(*s, dtype=torch.bfloat16) * 0.05)(Dff, D)),
        }
    _B_in = _B_setup()

    return {
        "A_x": _A_in["x"],
        "A_wqkv": _A_in["wqkv"],
        "A_wo": _A_in["wo"],
        "B_x": _B_in["x"],
        "B_w1": _B_in["w1"],
        "B_w2": _B_in["w2"],
        "gate_w": _w((lambda *s: torch.randn(*s, dtype=torch.bfloat16) * 0.05)(512) * 16.0),
    }

def reference(A_x, A_wqkv, A_wo, B_x, B_w1, B_w2, gate_w):
    def _A_ref(x, wqkv, wo):
        B, S, D = x.shape
        H = 32
        Dh = D // H
        qkv = (lambda x, *d: x.permute(*d))((x @ wqkv).reshape(B, S, 3, H, Dh), 2, 0, 3, 1, 4)
        q, k, v = qkv[0], qkv[1], qkv[2]
        scores = torch.einsum("bhqd,bhkd->bhqk", q, k) * (Dh ** -0.5)
        p = _softmax(scores, axis=-1)
        o = torch.einsum("bhqk,bhkd->bhqd", p, v)
        return (lambda x, *d: x.permute(*d))(o, 0, 2, 1, 3).reshape(B, S, D) @ wo

    def _B_ref(x, w1, w2):
        h = torch.einsum("bsd,df->bsf", x, w1)
        zero = torch.zeros_like(h)
        h = torch.maximum(h, zero)
        return torch.einsum("bsf,fd->bsd", h, w2)

    x = A_x
    g = _sigmoid((x * gate_w).sum(-1, keepdims=True))
    a = _A_ref(x, A_wqkv, A_wo)
    b = _B_ref(x, B_w1, B_w2)
    return g * a + (1 - g) * b
```

```cpp
#include <hip/hip_runtime.h>
#include <hip/hip_bf16.h>
#include <stdint.h>

typedef __bf16 bf16x8 __attribute__((ext_vector_type(8)));
typedef __bf16 bf16x16 __attribute__((ext_vector_type(16)));
typedef __bf16 bf16x2 __attribute__((ext_vector_type(2)));
typedef short s16x8 __attribute__((ext_vector_type(8)));
typedef float f32x16 __attribute__((ext_vector_type(16)));
typedef float f32x8 __attribute__((ext_vector_type(8)));
typedef float f32x2 __attribute__((ext_vector_type(2)));
typedef unsigned u32x4 __attribute__((ext_vector_type(4)));
typedef unsigned long long u64;
#define LAS __attribute__((address_space(3)))
#define DI __device__ __forceinline__

constexpr int DFF = 1792;
constexpr unsigned WS_Q = 0, WS_K = 2u << 20, WS_V = 4u << 20, WS_O = 6u << 20, WS_H = 8u << 20, WS_G = 16u << 20, WS_F = 17u << 20, WS_END = 18u << 20;
constexpr unsigned FS = 128, F_E = WS_F, F_1 = WS_F + 4096, F_2 = F_1 + 1024 * FS, F_O = F_2 + 1024 * FS;
constexpr int VT = 1056, SLOT = 8192 + 8 * VT;
constexpr int L_Q = 9 * SLOT, L_G = L_Q + 8192, L_GW = L_G + 1024, L_CTR = L_GW + 1024, LDS_BYTES = L_CTR + 48;
constexpr int C_F = 0, C_X = 4, C_S = 8, C_K = 12, C_D = 24;
constexpr int RED_STRIDE = 272;
constexpr float QSCALE = 0.25f * 1.4426950408889634f;
constexpr unsigned CODE_BYTES = 27 * 1024;
#define KPOS(t) (((((t) >> 2) & 3) * 4 + ((t) >> 4)) * 4 + ((t) & 3))
constexpr unsigned SPIN_CAP = 1u << 18;

DI unsigned cvtpk(float lo, float hi) { f32x2 v = {lo, hi}; bf16x2 b = __builtin_convertvector(v, bf16x2); return __builtin_bit_cast(unsigned, b); }
DI f32x16 mfma(bf16x8 a, bf16x8 b, f32x16 c) { return __builtin_amdgcn_mfma_f32_32x32x16_bf16(a, b, c, 0, 0, 0); }
DI f32x16 smf(bf16x8 a, f32x8 p, f32x16 c, int idx) { return __builtin_amdgcn_smfmac_f32_32x32x32_bf16(a, __builtin_bit_cast(bf16x16, p), c, idx, 0, 0); }
DI f32x8 lo8(const f32x16& s) { return __builtin_shufflevector(s, s, 0, 1, 2, 3, 4, 5, 6, 7); }
DI f32x8 hi8(const f32x16& s) { return __builtin_shufflevector(s, s, 8, 9, 10, 11, 12, 13, 14, 15); }
DI void ldswrite(unsigned a, u32x4 v) { *(LAS u32x4*)(size_t)a = v; }
DI u32x4 pack8(const f32x16& a, int s, float sc) {
  return u32x4{cvtpk(a[8 * s] * sc, a[8 * s + 1] * sc), cvtpk(a[8 * s + 2] * sc, a[8 * s + 3] * sc), cvtpk(a[8 * s + 4] * sc, a[8 * s + 5] * sc), cvtpk(a[8 * s + 6] * sc, a[8 * s + 7] * sc)};
}
DI u32x4 max8(u32x4 v, short lo) { return __builtin_bit_cast(u32x4, __builtin_elementwise_max(__builtin_bit_cast(s16x8, v), s16x8{lo, lo, lo, lo, lo, lo, lo, lo})); }
DI float xhalf_max(float m) { auto rr = __builtin_amdgcn_permlane32_swap(__float_as_uint(m), __float_as_uint(m), false, false); return fmaxf(__uint_as_float(rr[0]), __uint_as_float(rr[1])); }
DI float xhalf_sum(float m) { auto rr = __builtin_amdgcn_permlane32_swap(__float_as_uint(m), __float_as_uint(m), false, false); return __uint_as_float(rr[0]) + __uint_as_float(rr[1]); }
DI void drain() { asm volatile("s_waitcnt vmcnt(0)" ::: "memory"); }
typedef __amdgpu_buffer_rsrc_t rsrc_t;
DI void st16(rsrc_t r, unsigned off, u32x4 v) { __builtin_amdgcn_raw_buffer_store_b128(v, r, off, 0, 16); }
DI void flag_set(rsrc_t r, unsigned off, u64 tagv, unsigned l8) {
  asm volatile("s_nop 3\n\ts_mov_b64 exec, 0xffff\n\tbuffer_store_dwordx2 %0, %3, %1, %2 offen sc1\n\ts_mov_b64 exec, -1" :: "v"(tagv), "s"(r), "s"(__builtin_amdgcn_readfirstlane(off)), "v"(l8) : "memory"); }
DI void wait_flags(rsrc_t r, unsigned voff, u64 tag) {
  u64 t; unsigned cap = SPIN_CAP;
  asm volatile("1:\n\tbuffer_load_dwordx2 %0, %2, %3, 0 offen sc1\n\ts_waitcnt vmcnt(0)\n\tv_cmp_ne_u64 vcc, %4, %0\n\ts_cbranch_vccz 2f\n\ts_sleep 2\n\t"
               "s_sub_u32 %1, %1, 1\n\ts_cmp_lg_u32 %1, 0\n\ts_cbranch_scc1 1b\n\t2:" : "=&v"(t), "+s"(cap) : "v"(voff), "s"(r), "s"(tag) : "scc", "vcc", "memory");
}
DI int fresh_lane() { int l; asm volatile("v_mbcnt_lo_u32_b32 %0, -1, 0\n\tv_mbcnt_hi_u32_b32 %0, -1, %0" : "=v"(l)); return l; }
DI void dma1(unsigned v0, const char* sbase, unsigned ldsdst) {
  asm volatile("s_mov_b32 m0, %1\n\ts_nop 0\n\tglobal_load_lds_dwordx4 %0, %2" :: "v"(v0), "s"(ldsdst), "s"(sbase) : "memory");
}
DI void dma_sc1(unsigned l16, rsrc_t r, unsigned soff, unsigned ldsdst) {
  asm volatile("s_mov_b32 m0, %3\n\ts_nop 0\n\tbuffer_load_dwordx4 %0, %1, %2 offen sc1 lds" :: "v"(l16), "s"(r), "s"(soff), "s"(ldsdst) : "memory");
}
template <int OFF> DI void lds_inc(unsigned cb, unsigned one) { asm volatile("s_mov_b64 exec, 1\n\tds_add_u32 %0, %1 offset:%2\n\ts_mov_b64 exec, -1" :: "v"(cb), "v"(one), "i"(OFF) : "memory"); }
template <int OFF, int NEED> DI void lds_wait(unsigned cb) {
  unsigned t, s;
  asm volatile("1:\n\tds_read_b32 %0, %2 offset:%3\n\ts_waitcnt lgkmcnt(0)\n\tv_readfirstlane_b32 %1, %0\n\ts_nop 3\n\ts_cmp_lt_u32 %1, %4\n\ts_cbranch_scc1 1b"
               : "=&v"(t), "=&s"(s) : "v"(cb), "i"(OFF), "i"(NEED) : "scc", "memory");
}
DI bf16x8 gfrag(rsrc_t r, unsigned off) { return __builtin_bit_cast(bf16x8, __builtin_amdgcn_raw_buffer_load_b128(r, off, 0, 16)); }
DI void attn_slow(f32x16& o, rsrc_t wsr, int hd, int qt, int lane) {
  const int r = lane & 31, h = lane >> 5;
  const bf16x8 qf = gfrag(wsr, WS_Q + (hd * 64 + qt) * 1024 + lane * 16), ones = __builtin_bit_cast(bf16x8, u32x4{0x3f803f80u, 0x3f803f80u, 0x3f803f80u, 0x3f803f80u});
  float m = -INFINITY; o = f32x16{};
#pragma unroll 1
  for (int t = 0; t < 64; ++t) {
    const unsigned vo = WS_V + (hd * 64 + t) * 1024 + (h * 16 + (r & 15)) * 16;
    const bf16x8 v0 = gfrag(wsr, vo), v1 = gfrag(wsr, vo + 512);
    f32x16 s = mfma(gfrag(wsr, WS_K + (hd * 64 + t) * 1024 + lane * 16), qf, f32x16{});
    float tm = s[0];
#pragma unroll
    for (int i = 1; i < 16; ++i) tm = fmaxf(tm, s[i]);
    const float mn = fmaxf(m, xhalf_max(tm));
    const float sc = __builtin_amdgcn_exp2f(m - mn);
    m = mn;
#pragma unroll
    for (int i = 0; i < 16; ++i) { o[i] *= sc; s[i] = __builtin_amdgcn_exp2f(s[i] - mn); }
    o = smf(r < 16 ? v0 : ones, lo8(s), o, 0xDDDDDDDD);
    o = smf(r < 16 ? v1 : ones, hi8(s), o, 0xDDDDDDDD);
  }
}

__global__ __launch_bounds__(512, 2) void fused(const char* __restrict__ x, const char* __restrict__ wqkv, const char* __restrict__ wo_, const char* __restrict__ w1,
                                                const char* __restrict__ w2, const char* __restrict__ gw, char* __restrict__ ws, char* __restrict__ out) {
  extern __shared__ __attribute__((aligned(16))) char lds[];
  const unsigned lds0 = (unsigned)(size_t)(LAS char*)lds;
  const int w = __builtin_amdgcn_readfirstlane(threadIdx.x >> 6);
  const int b = blockIdx.x, xcd = b & 7, bi = b >> 3;
  if (w == 0) {
    unsigned t_; const char* pc_;
    asm volatile("s_getpc_b64 %0" : "=s"(pc_));
    const unsigned o_ = min(((bi & 7) * 32 + (threadIdx.x & 31)) * 128, CODE_BYTES - 128);
    asm volatile("global_load_dword %0, %1, %2\n\ts_waitcnt vmcnt(0)" : "=v"(t_) : "v"(o_), "s"(pc_) : "memory");
  }
  asm volatile("" :: "s"(x), "s"(wqkv), "s"(wo_), "s"(w1), "s"(w2), "s"(gw), "s"(ws), "s"(out));
  const int rb = (xcd >> 1) * 4 + (bi & 3), cg = (bi >> 3) * 4 + (xcd & 1) * 2 + ((bi >> 2) & 1);
  const int job2 = cg < 14 ? cg : cg - 16;
  const int hd = xcd * 4 + (bi & 3), qb = bi >> 2;
  const int cgq = (hd >> 1) / 3, cgk = (16 + (hd >> 1)) / 3, cgv = (32 + (hd >> 1)) / 3;
  const int rt = w & 3, tt = rb * 4 + rt;
  const rsrc_t wsr = __builtin_amdgcn_make_buffer_rsrc(ws, 0, WS_END, 0x00020000);
  const unsigned ksrc = WS_K + hd * 65536 + rt * 2048;
#define ONES_WRITE(first_slot, n) ldswrite(lds0 + ((first_slot) + ((n) >> 4)) * SLOT + 8192 + (((n) >> 1) & 7) * VT + 1024 + ((n) & 1) * 16, u32x4{1u, 1u, 1u, 1u} * (((n) & 1) ? 0u : 0x3f803f80u))
#define XO0 ((32 * rt + r) * 128 + ((h ^ ((r >> 1) & 7)) << 4))
#define WO0 ((8 * h + ((lane & 15) >> 2)) * 256 + ((((((lane >> 4) & 1) << 1) | ((lane & 3) >> 1)) ^ ((lane & 12) | (2 * h))) << 4) + 8 * (lane & 1))
  const int tb = hd, nb = qb;
#define WOFF ((lane >> 3) * 1024 + (lane & 7) * 16)
#define WWR ((lane >> 3) * 128 + (((lane & 7) ^ ((lane >> 2) & 4)) << 4))
#define TR0 ((4 * h + ((lane & 15) >> 2)) * 128 + (((((lane >> 4) & 1) * 2 + ((lane & 3) >> 1)) ^ ((lane >> 1) & 4)) << 4) + 8 * (lane & 1))
#define OUT_OPERANDS(stg) [vw0] "v"(WOFF), [vw1] "v"(WOFF + 8192), [ww] "v"((stg) + WWR), [t0] "v"((stg) + TR0), [t1] "v"((stg) + (TR0 ^ 64)), \
    [rwr] "v"(lds0 + (w * 64 + r) * RED_STRIDE + h * 16), [rrd] "v"(lds0 + (w * 8 + (lane >> 3)) * RED_STRIDE + (lane & 7) * 32), [vo] "v"(WOFF), [cb] "v"(lds0 + L_CTR), \
    [ro] "s"(__builtin_amdgcn_make_buffer_rsrc((void*)wo_, 0, 512 * 1024, 0x00020000)), [ra] "s"(wsr), [tag] "s"(tag), \
    [rout] "s"(__builtin_amdgcn_make_buffer_rsrc(out + ((tb * 64 + w * 8) * 1024 + nb * 128), 0, 2048 * 1024 - ((tb * 64 + w * 8) * 1024 + nb * 128), 0x00020000))
  u64 tag;
  if (w < 4) {
    const int lane = fresh_lane(), r = lane & 31, h = lane >> 5;
    asm volatile("s_load_dwordx2 %0, %1, 0x0\n\ts_waitcnt lgkmcnt(0)" : "=s"(tag) : "s"(ws + F_E) : "memory");
    tag += 1;
    if (w == 0 && lane < 3) ldswrite(lds0 + L_CTR + lane * 16, u32x4{0u, 0u, 0u, 0u});
    if (w == 1 && lane < 16) ONES_WRITE(0, lane);
    __builtin_amdgcn_s_setprio(3);
    unsigned cb = lds0 + L_CTR, tmp; u64 t1;
    const int qt0 = qb * 8 + w * 2;
    const unsigned ka0 = lds0 + lane * 16;
    const unsigned vl0 = r < 16 ? (h * 16 + r) * 16 : (r & 27) == 16 ? 1024 : 1040;
    const unsigned va00 = lds0 + 8192 + vl0, va10 = va00 + (r < 16 ? 512 : 0);
    const unsigned fo = F_1 + (lane < 8 ? ((lane & 4) ? cgv : cgk) * 64 + KPOS((lane & 2) * 4 + 2 * rt + (lane & 1)) : cgq * 64 + qt0 + (lane & 1)) * FS;
    f32x16 o[2] = {}; float g[2];
    u64 tagv = tag;
    asm volatile("" : "+v"(o[0]), "+v"(o[1]), "+v"(tagv));
    {
      unsigned ph = 0, cnt = 7;
      const unsigned lq = lane * 16, lk = (((r & 19) | ((r & 4) << 1) | ((r & 8) >> 1)) + 32 * h) * 16, lv = (r >> 4) * 65536 + (h * 16 + (r & 15)) * 16;
      unsigned d[3];
#pragma unroll
      for (int u = 0; u < 3; ++u) { const int g = 3 * cg + u; d[u] = g * 131072 + (g < 16 ? tt * 1024 + lq : KPOS(tt) * 1024 + (g < 32 ? lk : lv)); }
#define JOB1_OPERANDS : [ph] "+s"(ph), [cnt] "+s"(cnt), [tmp] "=&s"(tmp), [t1] "=&s"(t1) \
      : [xo] "v"(XO0), [wo] "v"(WO0), [bx] "s"(lds0 + 2 * SLOT), [bw] "s"(lds0 + 6 * SLOT), [d0] "v"(d[0]), [d1] "v"(d[1]), [d2] "v"(d[2]), [ra] "s"(wsr), [k64] "s"(65536), \
        [qm] "s"(0xFFFFu >> min(3 * cg, 31)), [qs] "s"(QSCALE), [cb] "v"(cb), [fv] "v"((lane >> 4) * (DFF * 2) + ((lane & 15) ^ ((lane >> 4) << 2)) * 16), [rf] "s"(__builtin_amdgcn_make_buffer_rsrc((void*)(w1 + rt * 16 * (DFF * 2) + max(job2, 0) * 256), 0, 512 * DFF * 2 - rt * 16 * (DFF * 2) - max(job2, 0) * 256, 0x00020000)), [dst] "s"(lds0 + rt * 4096), [r0] "s"(((bi >> 2) + 2 * rb) & 7), [w1at] "s"(4 | (job2 >> 8)) : "v64", "v65", "v66", "v67", "v68", "v69", "v70", "v71", "v72", "v73", "v74", "v75", "v76", "v77", "v78", "v79", "v80", "v81", "v82", "v83", "v84", "v85", "v86", "v87", "v88", "v89", "v90", "v91", "v92", "v93", "v94", "v95", "v96", "v97", "v98", "v99", "v100", "v101", "v102", "v103", "v104", "v105", "v106", "v107", "v108", "v109", "v110", "v111", "v112", "v113", "v114", "v115", "v116", "v117", "v118", "v119", "v120", "v121", "v122", "v123", "v124", "v125", "v126", "v127", "v128", "v129", "v130", "v131", "v132", "v133", "v134", "v135", "v136", "v137", "v138", "v139", "v140", "v141", "v142", "v143", "v144", "v145", "v146", "v147", "v148", "v149", "v150", "v151", "v152", "v153", "v154", "v155", "v156", "v157", "v158", "v159", "v160", "v161", "v162", "v163", "v164", "v165", "v166", "v167", "v168", "v169", "v170", "v171", "v172", "v173", "v174", "v175", "v176", "v177", "v178", "v179", "v180", "v181", "v182", "v183", "v184", "v185", "v186", "v187", "v188", "v189", "v190", "v191", "v192", "v193", "v194", "v195", "v196", "v197", "v198", "v199", "v200", "v201", "v202", "v203", "v204", "v205", "v206", "v207", "v208", "v209", "v210", "v211", "v212", "v213", "v214", "v215", "scc", "memory"
      if (cg < 10) asm volatile(
      "v_xor_b32 v112, 0, %[xo]\n\t"
      "v_add_u32 v112, %[bx], v112\n\t"
      "v_xor_b32 v113, 32, %[xo]\n\t"
      "v_add_u32 v113, %[bx], v113\n\t"
      "v_xor_b32 v114, 64, %[xo]\n\t"
      "v_add_u32 v114, %[bx], v114\n\t"
      "v_xor_b32 v115, 96, %[xo]\n\t"
      "v_add_u32 v115, %[bx], v115\n\t"
      "v_xor_b32 v116, 0, %[wo]\n\t"
      "v_add_u32 v116, %[bw], v116\n\t"
      "v_xor_b32 v117, 16, %[wo]\n\t"
      "v_add_u32 v117, %[bw], v117\n\t"
      "v_xor_b32 v118, 64, %[wo]\n\t"
      "v_add_u32 v118, %[bw], v118\n\t"
      "v_xor_b32 v119, 80, %[wo]\n\t"
      "v_add_u32 v119, %[bw], v119\n\t"
      "v_xor_b32 v120, 128, %[wo]\n\t"
      "v_add_u32 v120, %[bw], v120\n\t"
      "v_xor_b32 v121, 144, %[wo]\n\t"
      "v_add_u32 v121, %[bw], v121\n\t"
      "v_mov_b32 v64, 0\n\t"
      "v_mov_b32 v65, 0\n\t"
      "v_mov_b32 v66, 0\n\t"
      "v_mov_b32 v67, 0\n\t"
      "v_mov_b32 v68, 0\n\t"
      "v_mov_b32 v69, 0\n\t"
      "v_mov_b32 v70, 0\n\t"
      "v_mov_b32 v71, 0\n\t"
      "v_mov_b32 v72, 0\n\t"
      "v_mov_b32 v73, 0\n\t"
      "v_mov_b32 v74, 0\n\t"
      "v_mov_b32 v75, 0\n\t"
      "v_mov_b32 v76, 0\n\t"
      "v_mov_b32 v77, 0\n\t"
      "v_mov_b32 v78, 0\n\t"
      "v_mov_b32 v79, 0\n\t"
      "v_mov_b32 v80, 0\n\t"
      "v_mov_b32 v81, 0\n\t"
      "v_mov_b32 v82, 0\n\t"
      "v_mov_b32 v83, 0\n\t"
      "v_mov_b32 v84, 0\n\t"
      "v_mov_b32 v85, 0\n\t"
      "v_mov_b32 v86, 0\n\t"
      "v_mov_b32 v87, 0\n\t"
      "v_mov_b32 v88, 0\n\t"
      "v_mov_b32 v89, 0\n\t"
      "v_mov_b32 v90, 0\n\t"
      "v_mov_b32 v91, 0\n\t"
      "v_mov_b32 v92, 0\n\t"
      "v_mov_b32 v93, 0\n\t"
      "v_mov_b32 v94, 0\n\t"
      "v_mov_b32 v95, 0\n\t"
      "v_mov_b32 v96, 0\n\t"
      "v_mov_b32 v97, 0\n\t"
      "v_mov_b32 v98, 0\n\t"
      "v_mov_b32 v99, 0\n\t"
      "v_mov_b32 v100, 0\n\t"
      "v_mov_b32 v101, 0\n\t"
      "v_mov_b32 v102, 0\n\t"
      "v_mov_b32 v103, 0\n\t"
      "v_mov_b32 v104, 0\n\t"
      "v_mov_b32 v105, 0\n\t"
      "v_mov_b32 v106, 0\n\t"
      "v_mov_b32 v107, 0\n\t"
      "v_mov_b32 v108, 0\n\t"
      "v_mov_b32 v109, 0\n\t"
      "v_mov_b32 v110, 0\n\t"
      "v_mov_b32 v111, 0\n\t"
      "v_mov_b32 v122, 1\n\t"
      "v_xor_b32 v123, 0, %[fv]\n\t"
      "v_add_u32 v123, 0, v123\n\t"
      "v_xor_b32 v124, 16, %[fv]\n\t"
      "v_add_u32 v124, 13312, v124\n\t"
      "v_xor_b32 v125, 32, %[fv]\n\t"
      "v_add_u32 v125, 26624, v125\n\t"
      "v_xor_b32 v126, 48, %[fv]\n\t"
      "v_add_u32 v126, 39936, v126\n\t"
      "s_waitcnt lgkmcnt(0)\n\t"
      "s_barrier\n\t"
      "s_memrealtime %[t1]\n\t"
      "s_waitcnt lgkmcnt(0)\n\t"
      "s_branch 2f\n\t"
      "1:\n\t"
      "s_cmp_lg_u32 %[cnt], %[w1at]\n\t"
      "s_cbranch_scc1 3f\n\t"
      "s_add_u32 %[tmp], %[r0], 0\n\t"
      "s_and_b32 %[tmp], %[tmp], 7\n\t"
      "s_mul_i32 %[tmp], %[tmp], 229376\n\t"
      "s_add_u32 m0, %[dst], 83200\n\t"
      "s_nop 0\n\t"
      "buffer_load_dwordx4 v123, %[rf], %[tmp] offen offset:0 lds\n\t"
      "buffer_load_dwordx4 v124, %[rf], %[tmp] offen offset:1024 lds\n\t"
      "buffer_load_dwordx4 v125, %[rf], %[tmp] offen offset:2048 lds\n\t"
      "buffer_load_dwordx4 v126, %[rf], %[tmp] offen offset:3072 lds\n\t"
      "s_add_u32 %[tmp], %[r0], 1\n\t"
      "s_and_b32 %[tmp], %[tmp], 7\n\t"
      "s_mul_i32 %[tmp], %[tmp], 229376\n\t"
      "s_add_u32 m0, %[dst], 16640\n\t"
      "s_nop 0\n\t"
      "buffer_load_dwordx4 v123, %[rf], %[tmp] offen offset:0 lds\n\t"
      "buffer_load_dwordx4 v124, %[rf], %[tmp] offen offset:1024 lds\n\t"
      "buffer_load_dwordx4 v125, %[rf], %[tmp] offen offset:2048 lds\n\t"
      "buffer_load_dwordx4 v126, %[rf], %[tmp] offen offset:3072 lds\n\t"
      "3:\n\t"
      "s_barrier\n\t"
      "2:\n\t"
      "ds_read_b128 v[128:131], v112\n\t"
      "ds_read_b64_tr_b16 v[144:145], v116 offset:0\n\t"
      "ds_read_b64_tr_b16 v[146:147], v117 offset:1024\n\t"
      "ds_read_b64_tr_b16 v[148:149], v118 offset:0\n\t"
      "ds_read_b64_tr_b16 v[150:151], v119 offset:1024\n\t"
      "ds_read_b64_tr_b16 v[152:153], v120 offset:0\n\t"
      "ds_read_b64_tr_b16 v[154:155], v121 offset:1024\n\t"
      "ds_read_b128 v[132:135], v113\n\t"
      "ds_read_b64_tr_b16 v[156:157], v116 offset:4096\n\t"
      "ds_read_b64_tr_b16 v[158:159], v117 offset:5120\n\t"
      "ds_read_b64_tr_b16 v[160:161], v118 offset:4096\n\t"
      "ds_read_b64_tr_b16 v[162:163], v119 offset:5120\n\t"
      "ds_read_b64_tr_b16 v[164:165], v120 offset:4096\n\t"
      "ds_read_b64_tr_b16 v[166:167], v121 offset:5120\n\t"
      "s_waitcnt lgkmcnt(7)\n\t"
      "v_mfma_f32_32x32x16_bf16 v[64:79], v[144:147], v[128:131], v[64:79]\n\t"
      "ds_read_b128 v[136:139], v114\n\t"
      "ds_read_b64_tr_b16 v[168:169], v116 offset:8192\n\t"
      "ds_read_b64_tr_b16 v[170:171], v117 offset:9216\n\t"
      "ds_read_b64_tr_b16 v[172:173], v118 offset:8192\n\t"
      "ds_read_b64_tr_b16 v[174:175], v119 offset:9216\n\t"
      "v_mfma_f32_32x32x16_bf16 v[80:95], v[148:151], v[128:131], v[80:95]\n\t"
      "ds_read_b64_tr_b16 v[176:177], v120 offset:8192\n\t"
      "ds_read_b64_tr_b16 v[178:179], v121 offset:9216\n\t"
      "ds_read_b128 v[140:143], v115\n\t"
      "ds_read_b64_tr_b16 v[180:181], v116 offset:12288\n\t"
      "ds_read_b64_tr_b16 v[182:183], v117 offset:13312\n\t"
      "v_mfma_f32_32x32x16_bf16 v[96:111], v[152:155], v[128:131], v[96:111]\n\t"
      "ds_read_b64_tr_b16 v[184:185], v118 offset:12288\n\t"
      "ds_read_b64_tr_b16 v[186:187], v119 offset:13312\n\t"
      "ds_read_b64_tr_b16 v[188:189], v120 offset:12288\n\t"
      "ds_read_b64_tr_b16 v[190:191], v121 offset:13312\n\t"
      "s_waitcnt lgkmcnt(14)\n\t"
      "v_mfma_f32_32x32x16_bf16 v[64:79], v[156:159], v[132:135], v[64:79]\n\t"
      "s_add_u32 %[ph], %[ph], 1\n\t"
      "s_cmp_eq_u32 %[ph], 3\n\t"
      "s_cselect_b32 %[ph], 0, %[ph]\n\t"
      "v_mfma_f32_32x32x16_bf16 v[80:95], v[160:163], v[132:135], v[80:95]\n\t"
      "s_mov_b32 %[tmp], 16640\n\t"
      "s_cselect_b32 %[tmp], -33280, %[tmp]\n\t"
      "v_add_u32 v112, %[tmp], v112\n\t"
      "v_mfma_f32_32x32x16_bf16 v[96:111], v[164:167], v[132:135], v[96:111]\n\t"
      "v_add_u32 v113, %[tmp], v113\n\t"
      "v_add_u32 v114, %[tmp], v114\n\t"
      "v_add_u32 v115, %[tmp], v115\n\t"
      "s_waitcnt lgkmcnt(7)\n\t"
      "v_mfma_f32_32x32x16_bf16 v[64:79], v[168:171], v[136:139], v[64:79]\n\t"
      "v_add_u32 v116, %[tmp], v116\n\t"
      "v_add_u32 v117, %[tmp], v117\n\t"
      "v_add_u32 v118, %[tmp], v118\n\t"
      "v_mfma_f32_32x32x16_bf16 v[80:95], v[172:175], v[136:139], v[80:95]\n\t"
      "v_add_u32 v119, %[tmp], v119\n\t"
      "v_add_u32 v120, %[tmp], v120\n\t"
      "v_add_u32 v121, %[tmp], v121\n\t"
      "v_mfma_f32_32x32x16_bf16 v[96:111], v[176:179], v[136:139], v[96:111]\n\t"
      "s_sub_u32 %[cnt], %[cnt], 1\n\t"
      "s_cmp_lg_u32 %[cnt], 0\n\t"
      "s_waitcnt lgkmcnt(0)\n\t"
      "v_mfma_f32_32x32x16_bf16 v[64:79], v[180:183], v[140:143], v[64:79]\n\t"
      "v_mfma_f32_32x32x16_bf16 v[80:95], v[184:187], v[140:143], v[80:95]\n\t"
      "v_mfma_f32_32x32x16_bf16 v[96:111], v[188:191], v[140:143], v[96:111]\n\t"
      "s_cbranch_scc1 1b\n\t"
      "s_waitcnt vmcnt(0)\n\t"
      "s_barrier\n\t"
      "ds_read_b128 v[128:131], v112\n\t"
      "ds_read_b64_tr_b16 v[144:145], v116 offset:0\n\t"
      "ds_read_b64_tr_b16 v[146:147], v117 offset:1024\n\t"
      "ds_read_b128 v[132:135], v113\n\t"
      "ds_read_b64_tr_b16 v[156:157], v116 offset:4096\n\t"
      "ds_read_b64_tr_b16 v[158:159], v117 offset:5120\n\t"
      "ds_read_b128 v[136:139], v114\n\t"
      "ds_read_b64_tr_b16 v[168:169], v116 offset:8192\n\t"
      "ds_read_b64_tr_b16 v[170:171], v117 offset:9216\n\t"
      "ds_read_b128 v[140:143], v115\n\t"
      "ds_read_b64_tr_b16 v[180:181], v116 offset:12288\n\t"
      "ds_read_b64_tr_b16 v[182:183], v117 offset:13312\n\t"
      "s_waitcnt lgkmcnt(9)\n\t"
      "v_mfma_f32_32x32x16_bf16 v[64:79], v[144:147], v[128:131], v[64:79]\n\t"
      "ds_read_b64_tr_b16 v[148:149], v118 offset:0\n\t"
      "ds_read_b64_tr_b16 v[150:151], v119 offset:1024\n\t"
      "ds_read_b64_tr_b16 v[160:161], v118 offset:4096\n\t"
      "ds_read_b64_tr_b16 v[162:163], v119 offset:5120\n\t"
      "s_waitcnt lgkmcnt(10)\n\t"
      "v_mfma_f32_32x32x16_bf16 v[64:79], v[156:159], v[132:135], v[64:79]\n\t"
      "ds_read_b64_tr_b16 v[172:173], v118 offset:8192\n\t"
      "ds_read_b64_tr_b16 v[174:175], v119 offset:9216\n\t"
      "ds_read_b64_tr_b16 v[184:185], v118 offset:12288\n\t"
      "ds_read_b64_tr_b16 v[186:187], v119 offset:13312\n\t"
      "s_waitcnt lgkmcnt(11)\n\t"
      "v_mfma_f32_32x32x16_bf16 v[64:79], v[168:171], v[136:139], v[64:79]\n\t"
      "ds_read_b64_tr_b16 v[152:153], v120 offset:0\n\t"
      "ds_read_b64_tr_b16 v[154:155], v121 offset:1024\n\t"
      "ds_read_b64_tr_b16 v[164:165], v120 offset:4096\n\t"
      "ds_read_b64_tr_b16 v[166:167], v121 offset:5120\n\t"
      "s_waitcnt lgkmcnt(12)\n\t"
      "v_mfma_f32_32x32x16_bf16 v[64:79], v[180:183], v[140:143], v[64:79]\n\t"
      "ds_read_b64_tr_b16 v[176:177], v120 offset:8192\n\t"
      "ds_read_b64_tr_b16 v[178:179], v121 offset:9216\n\t"
      "ds_read_b64_tr_b16 v[188:189], v120 offset:12288\n\t"
      "ds_read_b64_tr_b16 v[190:191], v121 offset:13312\n\t"
      "s_waitcnt lgkmcnt(14)\n\t"
      "v_mfma_f32_32x32x16_bf16 v[80:95], v[148:151], v[128:131], v[80:95]\n\t"
      "s_nop 7\n\t"
      "s_waitcnt lgkmcnt(12)\n\t"
      "v_mfma_f32_32x32x16_bf16 v[80:95], v[160:163], v[132:135], v[80:95]\n\t"
      "s_nop 1\n\t"
      "s_waitcnt lgkmcnt(10)\n\t"
      "v_mfma_f32_32x32x16_bf16 v[80:95], v[172:175], v[136:139], v[80:95]\n\t"
      "s_bitcmp1_b32 %[qm], 0\n\t"
      "s_cbranch_scc0 10f\n\t"
      "v_mul_f32 v64, %[qs], v64\n\t"
      "v_mul_f32 v65, %[qs], v65\n\t"
      "v_mul_f32 v66, %[qs], v66\n\t"
      "v_mul_f32 v67, %[qs], v67\n\t"
      "v_mul_f32 v68, %[qs], v68\n\t"
      "v_mul_f32 v69, %[qs], v69\n\t"
      "v_mul_f32 v70, %[qs], v70\n\t"
      "v_mul_f32 v71, %[qs], v71\n\t"
      "v_mul_f32 v72, %[qs], v72\n\t"
      "v_mul_f32 v73, %[qs], v73\n\t"
      "v_mul_f32 v74, %[qs], v74\n\t"
      "v_mul_f32 v75, %[qs], v75\n\t"
      "v_mul_f32 v76, %[qs], v76\n\t"
      "v_mul_f32 v77, %[qs], v77\n\t"
      "v_mul_f32 v78, %[qs], v78\n\t"
      "v_mul_f32 v79, %[qs], v79\n\t"
      "10:\n\t"
      "s_waitcnt lgkmcnt(8)\n\t"
      "v_mfma_f32_32x32x16_bf16 v[80:95], v[184:187], v[140:143], v[80:95]\n\t"
      "v_cvt_pk_bf16_f32 v192, v64, v65\n\t"
      "v_cvt_pk_bf16_f32 v193, v66, v67\n\t"
      "v_cvt_pk_bf16_f32 v194, v68, v69\n\t"
      "v_cvt_pk_bf16_f32 v195, v70, v71\n\t"
      "buffer_store_dwordx4 v[192:195], %[d0], %[ra], 0 offen sc1\n\t"
      "s_waitcnt lgkmcnt(6)\n\t"
      "v_mfma_f32_32x32x16_bf16 v[96:111], v[152:155], v[128:131], v[96:111]\n\t"
      "v_cvt_pk_bf16_f32 v196, v72, v73\n\t"
      "v_cvt_pk_bf16_f32 v197, v74, v75\n\t"
      "v_cvt_pk_bf16_f32 v198, v76, v77\n\t"
      "v_cvt_pk_bf16_f32 v199, v78, v79\n\t"
      "buffer_store_dwordx4 v[196:199], %[d0], %[ra], %[k64] offen sc1\n\t"
      "s_waitcnt lgkmcnt(4)\n\t"
      "v_mfma_f32_32x32x16_bf16 v[96:111], v[164:167], v[132:135], v[96:111]\n\t"
      "s_nop 1\n\t"
      "s_waitcnt lgkmcnt(2)\n\t"
      "v_mfma_f32_32x32x16_bf16 v[96:111], v[176:179], v[136:139], v[96:111]\n\t"
      "s_bitcmp1_b32 %[qm], 1\n\t"
      "s_cbranch_scc0 11f\n\t"
      "v_mul_f32 v80, %[qs], v80\n\t"
      "v_mul_f32 v81, %[qs], v81\n\t"
      "v_mul_f32 v82, %[qs], v82\n\t"
      "v_mul_f32 v83, %[qs], v83\n\t"
      "v_mul_f32 v84, %[qs], v84\n\t"
      "v_mul_f32 v85, %[qs], v85\n\t"
      "v_mul_f32 v86, %[qs], v86\n\t"
      "v_mul_f32 v87, %[qs], v87\n\t"
      "v_mul_f32 v88, %[qs], v88\n\t"
      "v_mul_f32 v89, %[qs], v89\n\t"
      "v_mul_f32 v90, %[qs], v90\n\t"
      "v_mul_f32 v91, %[qs], v91\n\t"
      "v_mul_f32 v92, %[qs], v92\n\t"
      "v_mul_f32 v93, %[qs], v93\n\t"
      "v_mul_f32 v94, %[qs], v94\n\t"
      "v_mul_f32 v95, %[qs], v95\n\t"
      "11:\n\t"
      "s_waitcnt lgkmcnt(0)\n\t"
      "v_mfma_f32_32x32x16_bf16 v[96:111], v[188:191], v[140:143], v[96:111]\n\t"
      "v_cvt_pk_bf16_f32 v200, v80, v81\n\t"
      "v_cvt_pk_bf16_f32 v201, v82, v83\n\t"
      "v_cvt_pk_bf16_f32 v202, v84, v85\n\t"
      "v_cvt_pk_bf16_f32 v203, v86, v87\n\t"
      "buffer_store_dwordx4 v[200:203], %[d1], %[ra], 0 offen sc1\n\t"
      "v_cvt_pk_bf16_f32 v204, v88, v89\n\t"
      "v_cvt_pk_bf16_f32 v205, v90, v91\n\t"
      "v_cvt_pk_bf16_f32 v206, v92, v93\n\t"
      "v_cvt_pk_bf16_f32 v207, v94, v95\n\t"
      "buffer_store_dwordx4 v[204:207], %[d1], %[ra], %[k64] offen sc1\n\t"
      "s_mov_b64 exec, 1\n\t"
      "ds_add_u32 %[cb], v122 offset:4\n\t"
      "s_mov_b64 exec, -1\n\t"
      "s_nop 0\n\t"
      "s_bitcmp1_b32 %[qm], 2\n\t"
      "s_cbranch_scc0 12f\n\t"
      "v_mul_f32 v96, %[qs], v96\n\t"
      "v_mul_f32 v97, %[qs], v97\n\t"
      "v_mul_f32 v98, %[qs], v98\n\t"
      "v_mul_f32 v99, %[qs], v99\n\t"
      "v_mul_f32 v100, %[qs], v100\n\t"
      "v_mul_f32 v101, %[qs], v101\n\t"
      "v_mul_f32 v102, %[qs], v102\n\t"
      "v_mul_f32 v103, %[qs], v103\n\t"
      "v_mul_f32 v104, %[qs], v104\n\t"
      "v_mul_f32 v105, %[qs], v105\n\t"
      "v_mul_f32 v106, %[qs], v106\n\t"
      "v_mul_f32 v107, %[qs], v107\n\t"
      "v_mul_f32 v108, %[qs], v108\n\t"
      "v_mul_f32 v109, %[qs], v109\n\t"
      "v_mul_f32 v110, %[qs], v110\n\t"
      "v_mul_f32 v111, %[qs], v111\n\t"
      "12:\n\t"
      "v_cvt_pk_bf16_f32 v208, v96, v97\n\t"
      "v_cvt_pk_bf16_f32 v209, v98, v99\n\t"
      "v_cvt_pk_bf16_f32 v210, v100, v101\n\t"
      "v_cvt_pk_bf16_f32 v211, v102, v103\n\t"
      "buffer_store_dwordx4 v[208:211], %[d2], %[ra], 0 offen sc1\n\t"
      "v_cvt_pk_bf16_f32 v212, v104, v105\n\t"
      "v_cvt_pk_bf16_f32 v213, v106, v107\n\t"
      "v_cvt_pk_bf16_f32 v214, v108, v109\n\t"
      "v_cvt_pk_bf16_f32 v215, v110, v111\n\t"
      "buffer_store_dwordx4 v[212:215], %[d2], %[ra], %[k64] offen sc1\n\t"
      "s_setprio 0\n\t"
      "s_nop 0\n\t"
        JOB1_OPERANDS);
      else if (cg == 10) asm volatile(
      "v_xor_b32 v112, 0, %[xo]\n\t"
      "v_add_u32 v112, %[bx], v112\n\t"
      "v_xor_b32 v113, 32, %[xo]\n\t"
      "v_add_u32 v113, %[bx], v113\n\t"
      "v_xor_b32 v114, 64, %[xo]\n\t"
      "v_add_u32 v114, %[bx], v114\n\t"
      "v_xor_b32 v115, 96, %[xo]\n\t"
      "v_add_u32 v115, %[bx], v115\n\t"
      "v_xor_b32 v116, 0, %[wo]\n\t"
      "v_add_u32 v116, %[bw], v116\n\t"
      "v_xor_b32 v117, 16, %[wo]\n\t"
      "v_add_u32 v117, %[bw], v117\n\t"
      "v_xor_b32 v118, 64, %[wo]\n\t"
      "v_add_u32 v118, %[bw], v118\n\t"
      "v_xor_b32 v119, 80, %[wo]\n\t"
      "v_add_u32 v119, %[bw], v119\n\t"
      "v_xor_b32 v120, 128, %[wo]\n\t"
      "v_add_u32 v120, %[bw], v120\n\t"
      "v_xor_b32 v121, 144, %[wo]\n\t"
      "v_add_u32 v121, %[bw], v121\n\t"
      "v_mov_b32 v64, 0\n\t"
      "v_mov_b32 v65, 0\n\t"
      "v_mov_b32 v66, 0\n\t"
      "v_mov_b32 v67, 0\n\t"
      "v_mov_b32 v68, 0\n\t"
      "v_mov_b32 v69, 0\n\t"
      "v_mov_b32 v70, 0\n\t"
      "v_mov_b32 v71, 0\n\t"
      "v_mov_b32 v72, 0\n\t"
      "v_mov_b32 v73, 0\n\t"
      "v_mov_b32 v74, 0\n\t"
      "v_mov_b32 v75, 0\n\t"
      "v_mov_b32 v76, 0\n\t"
      "v_mov_b32 v77, 0\n\t"
      "v_mov_b32 v78, 0\n\t"
      "v_mov_b32 v79, 0\n\t"
      "v_mov_b32 v80, 0\n\t"
      "v_mov_b32 v81, 0\n\t"
      "v_mov_b32 v82, 0\n\t"
      "v_mov_b32 v83, 0\n\t"
      "v_mov_b32 v84, 0\n\t"
      "v_mov_b32 v85, 0\n\t"
      "v_mov_b32 v86, 0\n\t"
      "v_mov_b32 v87, 0\n\t"
      "v_mov_b32 v88, 0\n\t"
      "v_mov_b32 v89, 0\n\t"
      "v_mov_b32 v90, 0\n\t"
      "v_mov_b32 v91, 0\n\t"
      "v_mov_b32 v92, 0\n\t"
      "v_mov_b32 v93, 0\n\t"
      "v_mov_b32 v94, 0\n\t"
      "v_mov_b32 v95, 0\n\t"
      "v_mov_b32 v96, 0\n\t"
      "v_mov_b32 v97, 0\n\t"
      "v_mov_b32 v98, 0\n\t"
      "v_mov_b32 v99, 0\n\t"
      "v_mov_b32 v100, 0\n\t"
      "v_mov_b32 v101, 0\n\t"
      "v_mov_b32 v102, 0\n\t"
      "v_mov_b32 v103, 0\n\t"
      "v_mov_b32 v104, 0\n\t"
      "v_mov_b32 v105, 0\n\t"
      "v_mov_b32 v106, 0\n\t"
      "v_mov_b32 v107, 0\n\t"
      "v_mov_b32 v108, 0\n\t"
      "v_mov_b32 v109, 0\n\t"
      "v_mov_b32 v110, 0\n\t"
      "v_mov_b32 v111, 0\n\t"
      "v_mov_b32 v122, 1\n\t"
      "v_xor_b32 v123, 0, %[fv]\n\t"
      "v_add_u32 v123, 0, v123\n\t"
      "v_xor_b32 v124, 16, %[fv]\n\t"
      "v_add_u32 v124, 13312, v124\n\t"
      "v_xor_b32 v125, 32, %[fv]\n\t"
      "v_add_u32 v125, 26624, v125\n\t"
      "v_xor_b32 v126, 48, %[fv]\n\t"
      "v_add_u32 v126, 39936, v126\n\t"
      "s_waitcnt lgkmcnt(0)\n\t"
      "s_barrier\n\t"
      "s_memrealtime %[t1]\n\t"
      "s_waitcnt lgkmcnt(0)\n\t"
      "s_branch 2f\n\t"
      "1:\n\t"
      "s_cmp_lg_u32 %[cnt], %[w1at]\n\t"
      "s_cbranch_scc1 3f\n\t"
      "s_add_u32 %[tmp], %[r0], 0\n\t"
      "s_and_b32 %[tmp], %[tmp], 7\n\t"
      "s_mul_i32 %[tmp], %[tmp], 229376\n\t"
      "s_add_u32 m0, %[dst], 83200\n\t"
      "s_nop 0\n\t"
      "buffer_load_dwordx4 v123, %[rf], %[tmp] offen offset:0 lds\n\t"
      "buffer_load_dwordx4 v124, %[rf], %[tmp] offen offset:1024 lds\n\t"
      "buffer_load_dwordx4 v125, %[rf], %[tmp] offen offset:2048 lds\n\t"
      "buffer_load_dwordx4 v126, %[rf], %[tmp] offen offset:3072 lds\n\t"
      "s_add_u32 %[tmp], %[r0], 1\n\t"
      "s_and_b32 %[tmp], %[tmp], 7\n\t"
      "s_mul_i32 %[tmp], %[tmp], 229376\n\t"
      "s_add_u32 m0, %[dst], 16640\n\t"
      "s_nop 0\n\t"
      "buffer_load_dwordx4 v123, %[rf], %[tmp] offen offset:0 lds\n\t"
      "buffer_load_dwordx4 v124, %[rf], %[tmp] offen offset:1024 lds\n\t"
      "buffer_load_dwordx4 v125, %[rf], %[tmp] offen offset:2048 lds\n\t"
      "buffer_load_dwordx4 v126, %[rf], %[tmp] offen offset:3072 lds\n\t"
      "3:\n\t"
      "s_barrier\n\t"
      "2:\n\t"
      "ds_read_b128 v[128:131], v112\n\t"
      "ds_read_b64_tr_b16 v[144:145], v116 offset:0\n\t"
      "ds_read_b64_tr_b16 v[146:147], v117 offset:1024\n\t"
      "ds_read_b64_tr_b16 v[148:149], v118 offset:0\n\t"
      "ds_read_b64_tr_b16 v[150:151], v119 offset:1024\n\t"
      "ds_read_b64_tr_b16 v[152:153], v120 offset:0\n\t"
      "ds_read_b64_tr_b16 v[154:155], v121 offset:1024\n\t"
      "ds_read_b128 v[132:135], v113\n\t"
      "ds_read_b64_tr_b16 v[156:157], v116 offset:4096\n\t"
      "ds_read_b64_tr_b16 v[158:159], v117 offset:5120\n\t"
      "ds_read_b64_tr_b16 v[160:161], v118 offset:4096\n\t"
      "ds_read_b64_tr_b16 v[162:163], v119 offset:5120\n\t"
      "ds_read_b64_tr_b16 v[164:165], v120 offset:4096\n\t"
      "ds_read_b64_tr_b16 v[166:167], v121 offset:5120\n\t"
      "s_waitcnt lgkmcnt(7)\n\t"
      "v_mfma_f32_32x32x16_bf16 v[64:79], v[144:147], v[128:131], v[64:79]\n\t"
      "ds_read_b128 v[136:139], v114\n\t"
      "ds_read_b64_tr_b16 v[168:169], v116 offset:8192\n\t"
      "ds_read_b64_tr_b16 v[170:171], v117 offset:9216\n\t"
      "ds_read_b64_tr_b16 v[172:173], v118 offset:8192\n\t"
      "ds_read_b64_tr_b16 v[174:175], v119 offset:9216\n\t"
      "v_mfma_f32_32x32x16_bf16 v[80:95], v[148:151], v[128:131], v[80:95]\n\t"
      "ds_read_b64_tr_b16 v[176:177], v120 offset:8192\n\t"
      "ds_read_b64_tr_b16 v[178:179], v121 offset:9216\n\t"
      "ds_read_b128 v[140:143], v115\n\t"
      "ds_read_b64_tr_b16 v[180:181], v116 offset:12288\n\t"
      "ds_read_b64_tr_b16 v[182:183], v117 offset:13312\n\t"
      "v_mfma_f32_32x32x16_bf16 v[96:111], v[128:131], v[152:155], v[96:111]\n\t"
      "ds_read_b64_tr_b16 v[184:185], v118 offset:12288\n\t"
      "ds_read_b64_tr_b16 v[186:187], v119 offset:13312\n\t"
      "ds_read_b64_tr_b16 v[188:189], v120 offset:12288\n\t"
      "ds_read_b64_tr_b16 v[190:191], v121 offset:13312\n\t"
      "s_waitcnt lgkmcnt(14)\n\t"
      "v_mfma_f32_32x32x16_bf16 v[64:79], v[156:159], v[132:135], v[64:79]\n\t"
      "s_add_u32 %[ph], %[ph], 1\n\t"
      "s_cmp_eq_u32 %[ph], 3\n\t"
      "s_cselect_b32 %[ph], 0, %[ph]\n\t"
      "v_mfma_f32_32x32x16_bf16 v[80:95], v[160:163], v[132:135], v[80:95]\n\t"
      "s_mov_b32 %[tmp], 16640\n\t"
      "s_cselect_b32 %[tmp], -33280, %[tmp]\n\t"
      "v_add_u32 v112, %[tmp], v112\n\t"
      "v_mfma_f32_32x32x16_bf16 v[96:111], v[132:135], v[164:167], v[96:111]\n\t"
      "v_add_u32 v113, %[tmp], v113\n\t"
      "v_add_u32 v114, %[tmp], v114\n\t"
      "v_add_u32 v115, %[tmp], v115\n\t"
      "s_waitcnt lgkmcnt(7)\n\t"
      "v_mfma_f32_32x32x16_bf16 v[64:79], v[168:171], v[136:139], v[64:79]\n\t"
      "v_add_u32 v116, %[tmp], v116\n\t"
      "v_add_u32 v117, %[tmp], v117\n\t"
      "v_add_u32 v118, %[tmp], v118\n\t"
      "v_mfma_f32_32x32x16_bf16 v[80:95], v[172:175], v[136:139], v[80:95]\n\t"
      "v_add_u32 v119, %[tmp], v119\n\t"
      "v_add_u32 v120, %[tmp], v120\n\t"
      "v_add_u32 v121, %[tmp], v121\n\t"
      "v_mfma_f32_32x32x16_bf16 v[96:111], v[136:139], v[176:179], v[96:111]\n\t"
      "s_sub_u32 %[cnt], %[cnt], 1\n\t"
      "s_cmp_lg_u32 %[cnt], 0\n\t"
      "s_waitcnt lgkmcnt(0)\n\t"
      "v_mfma_f32_32x32x16_bf16 v[64:79], v[180:183], v[140:143], v[64:79]\n\t"
      "v_mfma_f32_32x32x16_bf16 v[80:95], v[184:187], v[140:143], v[80:95]\n\t"
      "v_mfma_f32_32x32x16_bf16 v[96:111], v[140:143], v[188:191], v[96:111]\n\t"
      "s_cbranch_scc1 1b\n\t"
      "s_waitcnt vmcnt(0)\n\t"
      "s_barrier\n\t"
      "ds_read_b128 v[128:131], v112\n\t"
      "ds_read_b64_tr_b16 v[144:145], v116 offset:0\n\t"
      "ds_read_b64_tr_b16 v[146:147], v117 offset:1024\n\t"
      "ds_read_b128 v[132:135], v113\n\t"
      "ds_read_b64_tr_b16 v[156:157], v116 offset:4096\n\t"
      "ds_read_b64_tr_b16 v[158:159], v117 offset:5120\n\t"
      "ds_read_b128 v[136:139], v114\n\t"
      "ds_read_b64_tr_b16 v[168:169], v116 offset:8192\n\t"
      "ds_read_b64_tr_b16 v[170:171], v117 offset:9216\n\t"
      "ds_read_b128 v[140:143], v115\n\t"
      "ds_read_b64_tr_b16 v[180:181], v116 offset:12288\n\t"
      "ds_read_b64_tr_b16 v[182:183], v117 offset:13312\n\t"
      "s_waitcnt lgkmcnt(9)\n\t"
      "v_mfma_f32_32x32x16_bf16 v[64:79], v[144:147], v[128:131], v[64:79]\n\t"
      "ds_read_b64_tr_b16 v[148:149], v118 offset:0\n\t"
      "ds_read_b64_tr_b16 v[150:151], v119 offset:1024\n\t"
      "ds_read_b64_tr_b16 v[160:161], v118 offset:4096\n\t"
      "ds_read_b64_tr_b16 v[162:163], v119 offset:5120\n\t"
      "s_waitcnt lgkmcnt(10)\n\t"
      "v_mfma_f32_32x32x16_bf16 v[64:79], v[156:159], v[132:135], v[64:79]\n\t"
      "ds_read_b64_tr_b16 v[172:173], v118 offset:8192\n\t"
      "ds_read_b64_tr_b16 v[174:175], v119 offset:9216\n\t"
      "ds_read_b64_tr_b16 v[184:185], v118 offset:12288\n\t"
      "ds_read_b64_tr_b16 v[186:187], v119 offset:13312\n\t"
      "s_waitcnt lgkmcnt(11)\n\t"
      "v_mfma_f32_32x32x16_bf16 v[64:79], v[168:171], v[136:139], v[64:79]\n\t"
      "ds_read_b64_tr_b16 v[152:153], v120 offset:0\n\t"
      "ds_read_b64_tr_b16 v[154:155], v121 offset:1024\n\t"
      "ds_read_b64_tr_b16 v[164:165], v120 offset:4096\n\t"
      "ds_read_b64_tr_b16 v[166:167], v121 offset:5120\n\t"
      "s_waitcnt lgkmcnt(12)\n\t"
      "v_mfma_f32_32x32x16_bf16 v[64:79], v[180:183], v[140:143], v[64:79]\n\t"
      "ds_read_b64_tr_b16 v[176:177], v120 offset:8192\n\t"
      "ds_read_b64_tr_b16 v[178:179], v121 offset:9216\n\t"
      "ds_read_b64_tr_b16 v[188:189], v120 offset:12288\n\t"
      "ds_read_b64_tr_b16 v[190:191], v121 offset:13312\n\t"
      "s_waitcnt lgkmcnt(14)\n\t"
      "v_mfma_f32_32x32x16_bf16 v[80:95], v[148:151], v[128:131], v[80:95]\n\t"
      "s_nop 7\n\t"
      "s_waitcnt lgkmcnt(12)\n\t"
      "v_mfma_f32_32x32x16_bf16 v[80:95], v[160:163], v[132:135], v[80:95]\n\t"
      "s_nop 1\n\t"
      "s_waitcnt lgkmcnt(10)\n\t"
      "v_mfma_f32_32x32x16_bf16 v[80:95], v[172:175], v[136:139], v[80:95]\n\t"
      "v_cvt_pk_bf16_f32 v192, v64, v65\n\t"
      "v_cvt_pk_bf16_f32 v193, v66, v67\n\t"
      "v_cvt_pk_bf16_f32 v194, v68, v69\n\t"
      "v_cvt_pk_bf16_f32 v195, v70, v71\n\t"
      "buffer_store_dwordx4 v[192:195], %[d0], %[ra], 0 offen sc1\n\t"
      "s_waitcnt lgkmcnt(8)\n\t"
      "v_mfma_f32_32x32x16_bf16 v[80:95], v[184:187], v[140:143], v[80:95]\n\t"
      "v_cvt_pk_bf16_f32 v196, v72, v73\n\t"
      "v_cvt_pk_bf16_f32 v197, v74, v75\n\t"
      "v_cvt_pk_bf16_f32 v198, v76, v77\n\t"
      "v_cvt_pk_bf16_f32 v199, v78, v79\n\t"
      "buffer_store_dwordx4 v[196:199], %[d0], %[ra], %[k64] offen sc1\n\t"
      "s_waitcnt lgkmcnt(6)\n\t"
      "v_mfma_f32_32x32x16_bf16 v[96:111], v[128:131], v[152:155], v[96:111]\n\t"
      "s_nop 7\n\t"
      "s_waitcnt lgkmcnt(4)\n\t"
      "v_mfma_f32_32x32x16_bf16 v[96:111], v[132:135], v[164:167], v[96:111]\n\t"
      "s_nop 1\n\t"
      "s_waitcnt lgkmcnt(2)\n\t"
      "v_mfma_f32_32x32x16_bf16 v[96:111], v[136:139], v[176:179], v[96:111]\n\t"
      "v_cvt_pk_bf16_f32 v200, v80, v81\n\t"
      "v_cvt_pk_bf16_f32 v201, v82, v83\n\t"
      "v_cvt_pk_bf16_f32 v202, v84, v85\n\t"
      "v_cvt_pk_bf16_f32 v203, v86, v87\n\t"
      "buffer_store_dwordx4 v[200:203], %[d1], %[ra], 0 offen sc1\n\t"
      "s_waitcnt lgkmcnt(0)\n\t"
      "v_mfma_f32_32x32x16_bf16 v[96:111], v[140:143], v[188:191], v[96:111]\n\t"
      "v_cvt_pk_bf16_f32 v204, v88, v89\n\t"
      "v_cvt_pk_bf16_f32 v205, v90, v91\n\t"
      "v_cvt_pk_bf16_f32 v206, v92, v93\n\t"
      "v_cvt_pk_bf16_f32 v207, v94, v95\n\t"
      "buffer_store_dwordx4 v[204:207], %[d1], %[ra], %[k64] offen sc1\n\t"
      "s_mov_b64 exec, 1\n\t"
      "ds_add_u32 %[cb], v122 offset:4\n\t"
      "s_mov_b64 exec, -1\n\t"
      "s_nop 4\n\t"
      "v_cvt_pk_bf16_f32 v208, v96, v97\n\t"
      "v_cvt_pk_bf16_f32 v209, v98, v99\n\t"
      "v_cvt_pk_bf16_f32 v210, v100, v101\n\t"
      "v_cvt_pk_bf16_f32 v211, v102, v103\n\t"
      "buffer_store_dwordx4 v[208:211], %[d2], %[ra], 0 offen sc1\n\t"
      "v_cvt_pk_bf16_f32 v212, v104, v105\n\t"
      "v_cvt_pk_bf16_f32 v213, v106, v107\n\t"
      "v_cvt_pk_bf16_f32 v214, v108, v109\n\t"
      "v_cvt_pk_bf16_f32 v215, v110, v111\n\t"
      "buffer_store_dwordx4 v[212:215], %[d2], %[ra], 0 offen offset:512 sc1\n\t"
      "s_setprio 0\n\t"
      "s_nop 0\n\t"
        JOB1_OPERANDS);
      else asm volatile(
      "v_xor_b32 v112, 0, %[xo]\n\t"
      "v_add_u32 v112, %[bx], v112\n\t"
      "v_xor_b32 v113, 32, %[xo]\n\t"
      "v_add_u32 v113, %[bx], v113\n\t"
      "v_xor_b32 v114, 64, %[xo]\n\t"
      "v_add_u32 v114, %[bx], v114\n\t"
      "v_xor_b32 v115, 96, %[xo]\n\t"
      "v_add_u32 v115, %[bx], v115\n\t"
      "v_xor_b32 v116, 0, %[wo]\n\t"
      "v_add_u32 v116, %[bw], v116\n\t"
      "v_xor_b32 v117, 16, %[wo]\n\t"
      "v_add_u32 v117, %[bw], v117\n\t"
      "v_xor_b32 v118, 64, %[wo]\n\t"
      "v_add_u32 v118, %[bw], v118\n\t"
      "v_xor_b32 v119, 80, %[wo]\n\t"
      "v_add_u32 v119, %[bw], v119\n\t"
      "v_xor_b32 v120, 128, %[wo]\n\t"
      "v_add_u32 v120, %[bw], v120\n\t"
      "v_xor_b32 v121, 144, %[wo]\n\t"
      "v_add_u32 v121, %[bw], v121\n\t"
      "v_mov_b32 v64, 0\n\t"
      "v_mov_b32 v65, 0\n\t"
      "v_mov_b32 v66, 0\n\t"
      "v_mov_b32 v67, 0\n\t"
      "v_mov_b32 v68, 0\n\t"
      "v_mov_b32 v69, 0\n\t"
      "v_mov_b32 v70, 0\n\t"
      "v_mov_b32 v71, 0\n\t"
      "v_mov_b32 v72, 0\n\t"
      "v_mov_b32 v73, 0\n\t"
      "v_mov_b32 v74, 0\n\t"
      "v_mov_b32 v75, 0\n\t"
      "v_mov_b32 v76, 0\n\t"
      "v_mov_b32 v77, 0\n\t"
      "v_mov_b32 v78, 0\n\t"
      "v_mov_b32 v79, 0\n\t"
      "v_mov_b32 v80, 0\n\t"
      "v_mov_b32 v81, 0\n\t"
      "v_mov_b32 v82, 0\n\t"
      "v_mov_b32 v83, 0\n\t"
      "v_mov_b32 v84, 0\n\t"
      "v_mov_b32 v85, 0\n\t"
      "v_mov_b32 v86, 0\n\t"
      "v_mov_b32 v87, 0\n\t"
      "v_mov_b32 v88, 0\n\t"
      "v_mov_b32 v89, 0\n\t"
      "v_mov_b32 v90, 0\n\t"
      "v_mov_b32 v91, 0\n\t"
      "v_mov_b32 v92, 0\n\t"
      "v_mov_b32 v93, 0\n\t"
      "v_mov_b32 v94, 0\n\t"
      "v_mov_b32 v95, 0\n\t"
      "v_mov_b32 v96, 0\n\t"
      "v_mov_b32 v97, 0\n\t"
      "v_mov_b32 v98, 0\n\t"
      "v_mov_b32 v99, 0\n\t"
      "v_mov_b32 v100, 0\n\t"
      "v_mov_b32 v101, 0\n\t"
      "v_mov_b32 v102, 0\n\t"
      "v_mov_b32 v103, 0\n\t"
      "v_mov_b32 v104, 0\n\t"
      "v_mov_b32 v105, 0\n\t"
      "v_mov_b32 v106, 0\n\t"
      "v_mov_b32 v107, 0\n\t"
      "v_mov_b32 v108, 0\n\t"
      "v_mov_b32 v109, 0\n\t"
      "v_mov_b32 v110, 0\n\t"
      "v_mov_b32 v111, 0\n\t"
      "v_mov_b32 v122, 1\n\t"
      "v_xor_b32 v123, 0, %[fv]\n\t"
      "v_add_u32 v123, 0, v123\n\t"
      "v_xor_b32 v124, 16, %[fv]\n\t"
      "v_add_u32 v124, 13312, v124\n\t"
      "v_xor_b32 v125, 32, %[fv]\n\t"
      "v_add_u32 v125, 26624, v125\n\t"
      "v_xor_b32 v126, 48, %[fv]\n\t"
      "v_add_u32 v126, 39936, v126\n\t"
      "s_waitcnt lgkmcnt(0)\n\t"
      "s_barrier\n\t"
      "s_memrealtime %[t1]\n\t"
      "s_waitcnt lgkmcnt(0)\n\t"
      "s_branch 2f\n\t"
      "1:\n\t"
      "s_cmp_lg_u32 %[cnt], %[w1at]\n\t"
      "s_cbranch_scc1 3f\n\t"
      "s_add_u32 %[tmp], %[r0], 0\n\t"
      "s_and_b32 %[tmp], %[tmp], 7\n\t"
      "s_mul_i32 %[tmp], %[tmp], 229376\n\t"
      "s_add_u32 m0, %[dst], 83200\n\t"
      "s_nop 0\n\t"
      "buffer_load_dwordx4 v123, %[rf], %[tmp] offen offset:0 lds\n\t"
      "buffer_load_dwordx4 v124, %[rf], %[tmp] offen offset:1024 lds\n\t"
      "buffer_load_dwordx4 v125, %[rf], %[tmp] offen offset:2048 lds\n\t"
      "buffer_load_dwordx4 v126, %[rf], %[tmp] offen offset:3072 lds\n\t"
      "s_add_u32 %[tmp], %[r0], 1\n\t"
      "s_and_b32 %[tmp], %[tmp], 7\n\t"
      "s_mul_i32 %[tmp], %[tmp], 229376\n\t"
      "s_add_u32 m0, %[dst], 16640\n\t"
      "s_nop 0\n\t"
      "buffer_load_dwordx4 v123, %[rf], %[tmp] offen offset:0 lds\n\t"
      "buffer_load_dwordx4 v124, %[rf], %[tmp] offen offset:1024 lds\n\t"
      "buffer_load_dwordx4 v125, %[rf], %[tmp] offen offset:2048 lds\n\t"
      "buffer_load_dwordx4 v126, %[rf], %[tmp] offen offset:3072 lds\n\t"
      "3:\n\t"
      "s_barrier\n\t"
      "2:\n\t"
      "ds_read_b128 v[128:131], v112\n\t"
      "ds_read_b64_tr_b16 v[144:145], v116 offset:0\n\t"
      "ds_read_b64_tr_b16 v[146:147], v117 offset:1024\n\t"
      "ds_read_b64_tr_b16 v[148:149], v118 offset:0\n\t"
      "ds_read_b64_tr_b16 v[150:151], v119 offset:1024\n\t"
      "ds_read_b64_tr_b16 v[152:153], v120 offset:0\n\t"
      "ds_read_b64_tr_b16 v[154:155], v121 offset:1024\n\t"
      "ds_read_b128 v[132:135], v113\n\t"
      "ds_read_b64_tr_b16 v[156:157], v116 offset:4096\n\t"
      "ds_read_b64_tr_b16 v[158:159], v117 offset:5120\n\t"
      "ds_read_b64_tr_b16 v[160:161], v118 offset:4096\n\t"
      "ds_read_b64_tr_b16 v[162:163], v119 offset:5120\n\t"
      "ds_read_b64_tr_b16 v[164:165], v120 offset:4096\n\t"
      "ds_read_b64_tr_b16 v[166:167], v121 offset:5120\n\t"
      "s_waitcnt lgkmcnt(7)\n\t"
      "v_mfma_f32_32x32x16_bf16 v[64:79], v[128:131], v[144:147], v[64:79]\n\t"
      "ds_read_b128 v[136:139], v114\n\t"
      "ds_read_b64_tr_b16 v[168:169], v116 offset:8192\n\t"
      "ds_read_b64_tr_b16 v[170:171], v117 offset:9216\n\t"
      "ds_read_b64_tr_b16 v[172:173], v118 offset:8192\n\t"
      "ds_read_b64_tr_b16 v[174:175], v119 offset:9216\n\t"
      "v_mfma_f32_32x32x16_bf16 v[80:95], v[128:131], v[148:151], v[80:95]\n\t"
      "ds_read_b64_tr_b16 v[176:177], v120 offset:8192\n\t"
      "ds_read_b64_tr_b16 v[178:179], v121 offset:9216\n\t"
      "ds_read_b128 v[140:143], v115\n\t"
      "ds_read_b64_tr_b16 v[180:181], v116 offset:12288\n\t"
      "ds_read_b64_tr_b16 v[182:183], v117 offset:13312\n\t"
      "v_mfma_f32_32x32x16_bf16 v[96:111], v[128:131], v[152:155], v[96:111]\n\t"
      "ds_read_b64_tr_b16 v[184:185], v118 offset:12288\n\t"
      "ds_read_b64_tr_b16 v[186:187], v119 offset:13312\n\t"
      "ds_read_b64_tr_b16 v[188:189], v120 offset:12288\n\t"
      "ds_read_b64_tr_b16 v[190:191], v121 offset:13312\n\t"
      "s_waitcnt lgkmcnt(14)\n\t"
      "v_mfma_f32_32x32x16_bf16 v[64:79], v[132:135], v[156:159], v[64:79]\n\t"
      "s_add_u32 %[ph], %[ph], 1\n\t"
      "s_cmp_eq_u32 %[ph], 3\n\t"
      "s_cselect_b32 %[ph], 0, %[ph]\n\t"
      "v_mfma_f32_32x32x16_bf16 v[80:95], v[132:135], v[160:163], v[80:95]\n\t"
      "s_mov_b32 %[tmp], 16640\n\t"
      "s_cselect_b32 %[tmp], -33280, %[tmp]\n\t"
      "v_add_u32 v112, %[tmp], v112\n\t"
      "v_mfma_f32_32x32x16_bf16 v[96:111], v[132:135], v[164:167], v[96:111]\n\t"
      "v_add_u32 v113, %[tmp], v113\n\t"
      "v_add_u32 v114, %[tmp], v114\n\t"
      "v_add_u32 v115, %[tmp], v115\n\t"
      "s_waitcnt lgkmcnt(7)\n\t"
      "v_mfma_f32_32x32x16_bf16 v[64:79], v[136:139], v[168:171], v[64:79]\n\t"
      "v_add_u32 v116, %[tmp], v116\n\t"
      "v_add_u32 v117, %[tmp], v117\n\t"
      "v_add_u32 v118, %[tmp], v118\n\t"
      "v_mfma_f32_32x32x16_bf16 v[80:95], v[136:139], v[172:175], v[80:95]\n\t"
      "v_add_u32 v119, %[tmp], v119\n\t"
      "v_add_u32 v120, %[tmp], v120\n\t"
      "v_add_u32 v121, %[tmp], v121\n\t"
      "v_mfma_f32_32x32x16_bf16 v[96:111], v[136:139], v[176:179], v[96:111]\n\t"
      "s_sub_u32 %[cnt], %[cnt], 1\n\t"
      "s_cmp_lg_u32 %[cnt], 0\n\t"
      "s_waitcnt lgkmcnt(0)\n\t"
      "v_mfma_f32_32x32x16_bf16 v[64:79], v[140:143], v[180:183], v[64:79]\n\t"
      "v_mfma_f32_32x32x16_bf16 v[80:95], v[140:143], v[184:187], v[80:95]\n\t"
      "v_mfma_f32_32x32x16_bf16 v[96:111], v[140:143], v[188:191], v[96:111]\n\t"
      "s_cbranch_scc1 1b\n\t"
      "s_waitcnt vmcnt(0)\n\t"
      "s_barrier\n\t"
      "ds_read_b128 v[128:131], v112\n\t"
      "ds_read_b64_tr_b16 v[144:145], v116 offset:0\n\t"
      "ds_read_b64_tr_b16 v[146:147], v117 offset:1024\n\t"
      "ds_read_b128 v[132:135], v113\n\t"
      "ds_read_b64_tr_b16 v[156:157], v116 offset:4096\n\t"
      "ds_read_b64_tr_b16 v[158:159], v117 offset:5120\n\t"
      "ds_read_b128 v[136:139], v114\n\t"
      "ds_read_b64_tr_b16 v[168:169], v116 offset:8192\n\t"
      "ds_read_b64_tr_b16 v[170:171], v117 offset:9216\n\t"
      "ds_read_b128 v[140:143], v115\n\t"
      "ds_read_b64_tr_b16 v[180:181], v116 offset:12288\n\t"
      "ds_read_b64_tr_b16 v[182:183], v117 offset:13312\n\t"
      "s_waitcnt lgkmcnt(9)\n\t"
      "v_mfma_f32_32x32x16_bf16 v[64:79], v[128:131], v[144:147], v[64:79]\n\t"
      "ds_read_b64_tr_b16 v[148:149], v118 offset:0\n\t"
      "ds_read_b64_tr_b16 v[150:151], v119 offset:1024\n\t"
      "ds_read_b64_tr_b16 v[160:161], v118 offset:4096\n\t"
      "ds_read_b64_tr_b16 v[162:163], v119 offset:5120\n\t"
      "s_waitcnt lgkmcnt(10)\n\t"
      "v_mfma_f32_32x32x16_bf16 v[64:79], v[132:135], v[156:159], v[64:79]\n\t"
      "ds_read_b64_tr_b16 v[172:173], v118 offset:8192\n\t"
      "ds_read_b64_tr_b16 v[174:175], v119 offset:9216\n\t"
      "ds_read_b64_tr_b16 v[184:185], v118 offset:12288\n\t"
      "ds_read_b64_tr_b16 v[186:187], v119 offset:13312\n\t"
      "s_waitcnt lgkmcnt(11)\n\t"
      "v_mfma_f32_32x32x16_bf16 v[64:79], v[136:139], v[168:171], v[64:79]\n\t"
      "ds_read_b64_tr_b16 v[152:153], v120 offset:0\n\t"
      "ds_read_b64_tr_b16 v[154:155], v121 offset:1024\n\t"
      "ds_read_b64_tr_b16 v[164:165], v120 offset:4096\n\t"
      "ds_read_b64_tr_b16 v[166:167], v121 offset:5120\n\t"
      "s_waitcnt lgkmcnt(12)\n\t"
      "v_mfma_f32_32x32x16_bf16 v[64:79], v[140:143], v[180:183], v[64:79]\n\t"
      "ds_read_b64_tr_b16 v[176:177], v120 offset:8192\n\t"
      "ds_read_b64_tr_b16 v[178:179], v121 offset:9216\n\t"
      "ds_read_b64_tr_b16 v[188:189], v120 offset:12288\n\t"
      "ds_read_b64_tr_b16 v[190:191], v121 offset:13312\n\t"
      "s_waitcnt lgkmcnt(14)\n\t"
      "v_mfma_f32_32x32x16_bf16 v[80:95], v[128:131], v[148:151], v[80:95]\n\t"
      "s_nop 7\n\t"
      "s_waitcnt lgkmcnt(12)\n\t"
      "v_mfma_f32_32x32x16_bf16 v[80:95], v[132:135], v[160:163], v[80:95]\n\t"
      "s_nop 1\n\t"
      "s_waitcnt lgkmcnt(10)\n\t"
      "v_mfma_f32_32x32x16_bf16 v[80:95], v[136:139], v[172:175], v[80:95]\n\t"
      "v_cvt_pk_bf16_f32 v192, v64, v65\n\t"
      "v_cvt_pk_bf16_f32 v193, v66, v67\n\t"
      "v_cvt_pk_bf16_f32 v194, v68, v69\n\t"
      "v_cvt_pk_bf16_f32 v195, v70, v71\n\t"
      "buffer_store_dwordx4 v[192:195], %[d0], %[ra], 0 offen sc1\n\t"
      "s_waitcnt lgkmcnt(8)\n\t"
      "v_mfma_f32_32x32x16_bf16 v[80:95], v[140:143], v[184:187], v[80:95]\n\t"
      "v_cvt_pk_bf16_f32 v196, v72, v73\n\t"
      "v_cvt_pk_bf16_f32 v197, v74, v75\n\t"
      "v_cvt_pk_bf16_f32 v198, v76, v77\n\t"
      "v_cvt_pk_bf16_f32 v199, v78, v79\n\t"
      "buffer_store_dwordx4 v[196:199], %[d0], %[ra], 0 offen offset:512 sc1\n\t"
      "s_waitcnt lgkmcnt(6)\n\t"
      "v_mfma_f32_32x32x16_bf16 v[96:111], v[128:131], v[152:155], v[96:111]\n\t"
      "s_nop 7\n\t"
      "s_waitcnt lgkmcnt(4)\n\t"
      "v_mfma_f32_32x32x16_bf16 v[96:111], v[132:135], v[164:167], v[96:111]\n\t"
      "s_nop 1\n\t"
      "s_waitcnt lgkmcnt(2)\n\t"
      "v_mfma_f32_32x32x16_bf16 v[96:111], v[136:139], v[176:179], v[96:111]\n\t"
      "v_cvt_pk_bf16_f32 v200, v80, v81\n\t"
      "v_cvt_pk_bf16_f32 v201, v82, v83\n\t"
      "v_cvt_pk_bf16_f32 v202, v84, v85\n\t"
      "v_cvt_pk_bf16_f32 v203, v86, v87\n\t"
      "buffer_store_dwordx4 v[200:203], %[d1], %[ra], 0 offen sc1\n\t"
      "s_waitcnt lgkmcnt(0)\n\t"
      "v_mfma_f32_32x32x16_bf16 v[96:111], v[140:143], v[188:191], v[96:111]\n\t"
      "v_cvt_pk_bf16_f32 v204, v88, v89\n\t"
      "v_cvt_pk_bf16_f32 v205, v90, v91\n\t"
      "v_cvt_pk_bf16_f32 v206, v92, v93\n\t"
      "v_cvt_pk_bf16_f32 v207, v94, v95\n\t"
      "buffer_store_dwordx4 v[204:207], %[d1], %[ra], 0 offen offset:512 sc1\n\t"
      "s_mov_b64 exec, 1\n\t"
      "ds_add_u32 %[cb], v122 offset:4\n\t"
      "s_mov_b64 exec, -1\n\t"
      "s_nop 4\n\t"
      "v_cvt_pk_bf16_f32 v208, v96, v97\n\t"
      "v_cvt_pk_bf16_f32 v209, v98, v99\n\t"
      "v_cvt_pk_bf16_f32 v210, v100, v101\n\t"
      "v_cvt_pk_bf16_f32 v211, v102, v103\n\t"
      "buffer_store_dwordx4 v[208:211], %[d2], %[ra], 0 offen sc1\n\t"
      "v_cvt_pk_bf16_f32 v212, v104, v105\n\t"
      "v_cvt_pk_bf16_f32 v213, v106, v107\n\t"
      "v_cvt_pk_bf16_f32 v214, v108, v109\n\t"
      "v_cvt_pk_bf16_f32 v215, v110, v111\n\t"
      "buffer_store_dwordx4 v[212:215], %[d2], %[ra], 0 offen offset:512 sc1\n\t"
      "s_setprio 0\n\t"
      "s_nop 0\n\t"
        JOB1_OPERANDS);
    }
    {
      unsigned ka = ka0, va0 = va00, va1 = va10, cnt = 8, cap = SPIN_CAP, cbk = cb;
      asm volatile(
      "v_mov_b32 v185, 1\n\t"
      "v_mov_b32 v228, 0xdddddddd\n\t"
      "s_waitcnt vmcnt(0)\n\t"
      "s_mov_b64 exec, 0xffff\n\t"
      "buffer_store_dwordx2 %[tv], %[l8], %[ra], %[f1] offen sc1\n\t"
      "s_mov_b64 exec, -1\n\t"
      "11:\n\t"
      "s_memrealtime s[88:89]\n\t"
      "s_waitcnt lgkmcnt(0)\n\t"
      "s_sub_u32 %[tmp], s88, %[t1]\n\t"
      "s_cmp_ge_u32 %[tmp], 530\n\t"
      "s_cbranch_scc1 12f\n\t"
      "s_sleep 2\n\t"
      "s_branch 11b\n\t"
      "12:\n\t"
      "s_mov_b64 exec, 1023\n\t"
      "6:\n\t"
      "buffer_load_dwordx2 v[186:187], %[fo], %[ra], 0 offen sc1\n\t"
      "s_waitcnt vmcnt(0)\n\t"
      "v_cmp_ne_u64 vcc, %[tag], v[186:187]\n\t"
      "s_cbranch_vccz 7f\n\t"
      "s_sleep 10\n\t"
      "s_sub_u32 %[cap], %[cap], 1\n\t"
      "s_cmp_lg_u32 %[cap], 0\n\t"
      "s_cbranch_scc1 6b\n\t"
      "7:\n\t"
      "s_mov_b64 exec, -1\n\t"
      "s_add_u32 m0, %[mq], 0\n\t"
      "s_add_u32 %[tmp], %[sq], 0\n\t"
      "buffer_load_dwordx4 %[l16], %[ra], %[tmp] offen sc1 lds\n\t"
      "s_add_u32 m0, %[mq], 1024\n\t"
      "s_add_u32 %[tmp], %[sq], 1024\n\t"
      "buffer_load_dwordx4 %[l16], %[ra], %[tmp] offen sc1 lds\n\t"
      "s_add_u32 m0, %[mk], 0\n\t"
      "s_add_u32 %[tmp], %[sk], 0\n\t"
      "buffer_load_dwordx4 %[l16], %[ra], %[tmp] offen sc1 lds\n\t"
      "s_add_u32 m0, %[mk], 1024\n\t"
      "s_add_u32 %[tmp], %[sk], 1024\n\t"
      "buffer_load_dwordx4 %[l16], %[ra], %[tmp] offen sc1 lds\n\t"
      "s_add_u32 m0, %[mv], 0\n\t"
      "s_add_u32 %[tmp], %[sk], 2097152\n\t"
      "buffer_load_dwordx4 %[l16], %[ra], %[tmp] offen sc1 lds\n\t"
      "s_add_u32 m0, %[mv], 1056\n\t"
      "s_add_u32 %[tmp], %[sk], 2098176\n\t"
      "buffer_load_dwordx4 %[l16], %[ra], %[tmp] offen sc1 lds\n\t"
      "10:\n\t"
      "ds_read_b32 v184, %[cb] offset:0\n\t"
      "s_waitcnt lgkmcnt(0)\n\t"
      "v_readfirstlane_b32 %[tmp], v184\n\t"
      "s_nop 3\n\t"
      "s_cmp_lt_u32 %[tmp], 12\n\t"
      "s_cbranch_scc1 10b\n\t"
      "s_mov_b64 exec, 15\n\t"
      "ds_write_b128 %[oa1], %[od]\n\t"
      "s_mov_b64 exec, -1\n\t"
      "s_add_u32 m0, %[mk], 16640\n\t"
      "s_add_u32 %[tmp], %[sk], 8192\n\t"
      "buffer_load_dwordx4 %[l16], %[ra], %[tmp] offen sc1 lds\n\t"
      "s_add_u32 m0, %[mk], 17664\n\t"
      "s_add_u32 %[tmp], %[sk], 9216\n\t"
      "buffer_load_dwordx4 %[l16], %[ra], %[tmp] offen sc1 lds\n\t"
      "s_add_u32 m0, %[mv], 16640\n\t"
      "s_add_u32 %[tmp], %[sk], 2105344\n\t"
      "buffer_load_dwordx4 %[l16], %[ra], %[tmp] offen sc1 lds\n\t"
      "s_add_u32 m0, %[mv], 17696\n\t"
      "s_add_u32 %[tmp], %[sk], 2106368\n\t"
      "buffer_load_dwordx4 %[l16], %[ra], %[tmp] offen sc1 lds\n\t"
      "v_add_u32 v186, %[mq], %[l16]\n\t"
      "v_add_u32 v187, %[mk], %[l16]\n\t"
      "s_waitcnt vmcnt(7)\n\t"
      "ds_read_b128 v[188:191], v186\n\t"
      "ds_read_b128 v[160:163], v187\n\t"
      "ds_read_b128 v[192:195], v186 offset:1024\n\t"
      "s_waitcnt lgkmcnt(1)\n\t"
      "v_mfma_f32_32x32x16_bf16 v[128:143], v[160:163], v[188:191], 0\n\t"
      "s_waitcnt lgkmcnt(0)\n\t"
      "v_mfma_f32_32x32x16_bf16 v[144:159], v[160:163], v[192:195], 0\n\t"
      "s_waitcnt vmcnt(4)\n\t"
      "s_mov_b64 exec, 1\n\t"
      "ds_add_u32 %[cb], v185 offset:8\n\t"
      "s_mov_b64 exec, -1\n\t"
      "s_nop 6\n\t"
      "v_max3_f32 v196, v128, v129, v130\n\t"
      "v_max3_f32 v197, v131, v132, v133\n\t"
      "v_max3_f32 v196, v196, v134, v135\n\t"
      "v_max3_f32 v197, v197, v136, v137\n\t"
      "v_max3_f32 v196, v196, v138, v139\n\t"
      "v_max3_f32 v197, v197, v140, v141\n\t"
      "v_max3_f32 v196, v196, v142, v143\n\t"
      "v_max_f32 v196, v196, v197\n\t"
      "v_mov_b32 v197, v196\n\t"
      "s_nop 1\n\t"
      "v_permlane32_swap_b32 v196, v197\n\t"
      "s_nop 1\n\t"
      "v_max_f32 v196, v196, v197\n\t"
      "v_xor_b32 v196, 0x80000000, v196\n\t"
      "v_mov_b32 v197, v196\n\t"
      "v_mov_b64 v[198:199], v[196:197]\n\t"
      "v_mov_b64 v[200:201], v[196:197]\n\t"
      "v_mov_b64 v[202:203], v[196:197]\n\t"
      "v_mov_b64 v[204:205], v[196:197]\n\t"
      "v_mov_b64 v[206:207], v[196:197]\n\t"
      "v_mov_b64 v[208:209], v[196:197]\n\t"
      "v_mov_b64 v[210:211], v[196:197]\n\t"
      "v_max3_f32 v212, v144, v145, v146\n\t"
      "v_max3_f32 v213, v147, v148, v149\n\t"
      "v_max3_f32 v212, v212, v150, v151\n\t"
      "v_max3_f32 v213, v213, v152, v153\n\t"
      "v_max3_f32 v212, v212, v154, v155\n\t"
      "v_max3_f32 v213, v213, v156, v157\n\t"
      "v_max3_f32 v212, v212, v158, v159\n\t"
      "v_max_f32 v212, v212, v213\n\t"
      "v_mov_b32 v213, v212\n\t"
      "s_nop 1\n\t"
      "v_permlane32_swap_b32 v212, v213\n\t"
      "s_nop 1\n\t"
      "v_max_f32 v212, v212, v213\n\t"
      "v_xor_b32 v212, 0x80000000, v212\n\t"
      "v_mov_b32 v213, v212\n\t"
      "v_mov_b64 v[214:215], v[212:213]\n\t"
      "v_mov_b64 v[216:217], v[212:213]\n\t"
      "v_mov_b64 v[218:219], v[212:213]\n\t"
      "v_mov_b64 v[220:221], v[212:213]\n\t"
      "v_mov_b64 v[222:223], v[212:213]\n\t"
      "v_mov_b64 v[224:225], v[212:213]\n\t"
      "v_mov_b64 v[226:227], v[212:213]\n\t"
      "v_mov_b64 v[152:153], 0\n\t"
      "v_mov_b64 v[154:155], 0\n\t"
      "v_mov_b64 v[156:157], 0\n\t"
      "v_mov_b64 v[158:159], 0\n\t"
      "8:\n\t"
      "ds_read_b32 v184, %[cb] offset:8\n\t"
      "s_waitcnt lgkmcnt(0)\n\t"
      "v_readfirstlane_b32 %[tmp], v184\n\t"
      "s_nop 3\n\t"
      "s_cmp_lt_u32 %[tmp], 4\n\t"
      "s_cbranch_scc1 8b\n\t"
      "ds_read_b128 v[160:163], %[ka] offset:0\n\t"
      "ds_read_b128 v[180:183], %[va0] offset:0\n\t"
      "ds_read_b128 v[168:171], %[va0] offset:0\n\t"
      "s_waitcnt lgkmcnt(2)\n\t"
      "v_mfma_f32_32x32x16_bf16 v[128:143], v[160:163], v[188:191], v[196:211]\n\t"
      "ds_read_b128 v[176:179], %[va1] offset:0\n\t"
      "ds_read_b128 v[164:167], %[ka] offset:1024\n\t"
      "ds_read_b128 v[172:175], %[va0] offset:1056\n\t"
      "s_waitcnt lgkmcnt(3)\n\t"
      "s_nop 7\n\t"
      "1:\n\t"
      "v_exp_f32 v128, v128\n\t"
      "v_exp_f32 v129, v129\n\t"
      "v_smfmac_f32_32x32x32_bf16 %[ob], v[180:183], v[152:159], v228\n\t"
      "v_exp_f32 v130, v130\n\t"
      "v_exp_f32 v131, v131\n\t"
      "v_exp_f32 v132, v132\n\t"
      "v_mfma_f32_32x32x16_bf16 v[144:159], v[160:163], v[192:195], v[212:227]\n\t"
      "s_waitcnt lgkmcnt(1)\n\t"
      "v_exp_f32 v133, v133\n\t"
      "v_exp_f32 v134, v134\n\t"
      "v_exp_f32 v135, v135\n\t"
      "v_exp_f32 v136, v136\n\t"
      "v_exp_f32 v137, v137\n\t"
      "v_smfmac_f32_32x32x32_bf16 %[oa], v[168:171], v[128:135], v228\n\t"
      "ds_read_b128 v[180:183], %[va1] offset:1056\n\t"
      "v_exp_f32 v138, v138\n\t"
      "v_exp_f32 v139, v139\n\t"
      "v_exp_f32 v140, v140\n\t"
      "v_exp_f32 v141, v141\n\t"
      "v_exp_f32 v142, v142\n\t"
      "v_exp_f32 v143, v143\n\t"
      "v_exp_f32 v144, v144\n\t"
      "v_exp_f32 v145, v145\n\t"
      "v_smfmac_f32_32x32x32_bf16 %[oa], v[176:179], v[136:143], v228\n\t"
      "v_exp_f32 v146, v146\n\t"
      "v_exp_f32 v147, v147\n\t"
      "v_exp_f32 v148, v148\n\t"
      "v_mfma_f32_32x32x16_bf16 v[128:143], v[164:167], v[188:191], v[196:211]\n\t"
      "ds_read_b128 v[160:163], %[ka] offset:2048\n\t"
      "v_exp_f32 v149, v149\n\t"
      "v_exp_f32 v150, v150\n\t"
      "v_exp_f32 v151, v151\n\t"
      "v_exp_f32 v152, v152\n\t"
      "v_exp_f32 v153, v153\n\t"
      "v_smfmac_f32_32x32x32_bf16 %[ob], v[168:171], v[144:151], v228\n\t"
      "ds_read_b128 v[168:171], %[va0] offset:2112\n\t"
      "v_exp_f32 v154, v154\n\t"
      "v_exp_f32 v155, v155\n\t"
      "v_exp_f32 v156, v156\n\t"
      "v_exp_f32 v157, v157\n\t"
      "v_exp_f32 v158, v158\n\t"
      "v_exp_f32 v159, v159\n\t"
      "v_exp_f32 v128, v128\n\t"
      "v_exp_f32 v129, v129\n\t"
      "v_smfmac_f32_32x32x32_bf16 %[ob], v[176:179], v[152:159], v228\n\t"
      "v_exp_f32 v130, v130\n\t"
      "v_exp_f32 v131, v131\n\t"
      "v_exp_f32 v132, v132\n\t"
      "v_mfma_f32_32x32x16_bf16 v[144:159], v[164:167], v[192:195], v[212:227]\n\t"
      "s_waitcnt lgkmcnt(1)\n\t"
      "v_exp_f32 v133, v133\n\t"
      "v_exp_f32 v134, v134\n\t"
      "v_exp_f32 v135, v135\n\t"
      "v_exp_f32 v136, v136\n\t"
      "v_exp_f32 v137, v137\n\t"
      "v_smfmac_f32_32x32x32_bf16 %[oa], v[172:175], v[128:135], v228\n\t"
      "ds_read_b128 v[176:179], %[va1] offset:2112\n\t"
      "v_exp_f32 v138, v138\n\t"
      "v_exp_f32 v139, v139\n\t"
      "v_exp_f32 v140, v140\n\t"
      "v_exp_f32 v141, v141\n\t"
      "v_exp_f32 v142, v142\n\t"
      "v_exp_f32 v143, v143\n\t"
      "v_exp_f32 v144, v144\n\t"
      "v_exp_f32 v145, v145\n\t"
      "v_smfmac_f32_32x32x32_bf16 %[oa], v[180:183], v[136:143], v228\n\t"
      "v_exp_f32 v146, v146\n\t"
      "v_exp_f32 v147, v147\n\t"
      "v_exp_f32 v148, v148\n\t"
      "v_mfma_f32_32x32x16_bf16 v[128:143], v[160:163], v[188:191], v[196:211]\n\t"
      "ds_read_b128 v[164:167], %[ka] offset:3072\n\t"
      "v_exp_f32 v149, v149\n\t"
      "v_exp_f32 v150, v150\n\t"
      "v_exp_f32 v151, v151\n\t"
      "v_exp_f32 v152, v152\n\t"
      "v_exp_f32 v153, v153\n\t"
      "v_smfmac_f32_32x32x32_bf16 %[ob], v[172:175], v[144:151], v228\n\t"
      "ds_read_b128 v[172:175], %[va0] offset:3168\n\t"
      "v_exp_f32 v154, v154\n\t"
      "v_exp_f32 v155, v155\n\t"
      "v_exp_f32 v156, v156\n\t"
      "v_exp_f32 v157, v157\n\t"
      "v_exp_f32 v158, v158\n\t"
      "v_exp_f32 v159, v159\n\t"
      "v_exp_f32 v128, v128\n\t"
      "v_exp_f32 v129, v129\n\t"
      "v_smfmac_f32_32x32x32_bf16 %[ob], v[180:183], v[152:159], v228\n\t"
      "v_exp_f32 v130, v130\n\t"
      "v_exp_f32 v131, v131\n\t"
      "v_exp_f32 v132, v132\n\t"
      "v_mfma_f32_32x32x16_bf16 v[144:159], v[160:163], v[192:195], v[212:227]\n\t"
      "s_waitcnt lgkmcnt(1)\n\t"
      "v_exp_f32 v133, v133\n\t"
      "v_exp_f32 v134, v134\n\t"
      "v_exp_f32 v135, v135\n\t"
      "v_exp_f32 v136, v136\n\t"
      "v_exp_f32 v137, v137\n\t"
      "v_smfmac_f32_32x32x32_bf16 %[oa], v[168:171], v[128:135], v228\n\t"
      "ds_read_b128 v[180:183], %[va1] offset:3168\n\t"
      "v_exp_f32 v138, v138\n\t"
      "v_exp_f32 v139, v139\n\t"
      "v_exp_f32 v140, v140\n\t"
      "v_exp_f32 v141, v141\n\t"
      "v_exp_f32 v142, v142\n\t"
      "v_exp_f32 v143, v143\n\t"
      "v_exp_f32 v144, v144\n\t"
      "v_exp_f32 v145, v145\n\t"
      "v_smfmac_f32_32x32x32_bf16 %[oa], v[176:179], v[136:143], v228\n\t"
      "v_exp_f32 v146, v146\n\t"
      "v_exp_f32 v147, v147\n\t"
      "v_exp_f32 v148, v148\n\t"
      "v_mfma_f32_32x32x16_bf16 v[128:143], v[164:167], v[188:191], v[196:211]\n\t"
      "ds_read_b128 v[160:163], %[ka] offset:4096\n\t"
      "v_exp_f32 v149, v149\n\t"
      "v_exp_f32 v150, v150\n\t"
      "v_exp_f32 v151, v151\n\t"
      "v_exp_f32 v152, v152\n\t"
      "v_exp_f32 v153, v153\n\t"
      "v_smfmac_f32_32x32x32_bf16 %[ob], v[168:171], v[144:151], v228\n\t"
      "ds_read_b128 v[168:171], %[va0] offset:4224\n\t"
      "v_exp_f32 v154, v154\n\t"
      "v_exp_f32 v155, v155\n\t"
      "v_exp_f32 v156, v156\n\t"
      "v_exp_f32 v157, v157\n\t"
      "v_exp_f32 v158, v158\n\t"
      "v_exp_f32 v159, v159\n\t"
      "v_exp_f32 v128, v128\n\t"
      "v_exp_f32 v129, v129\n\t"
      "v_smfmac_f32_32x32x32_bf16 %[ob], v[176:179], v[152:159], v228\n\t"
      "v_exp_f32 v130, v130\n\t"
      "v_exp_f32 v131, v131\n\t"
      "v_exp_f32 v132, v132\n\t"
      "v_mfma_f32_32x32x16_bf16 v[144:159], v[164:167], v[192:195], v[212:227]\n\t"
      "s_waitcnt lgkmcnt(1)\n\t"
      "v_exp_f32 v133, v133\n\t"
      "v_exp_f32 v134, v134\n\t"
      "v_exp_f32 v135, v135\n\t"
      "v_exp_f32 v136, v136\n\t"
      "v_exp_f32 v137, v137\n\t"
      "v_smfmac_f32_32x32x32_bf16 %[oa], v[172:175], v[128:135], v228\n\t"
      "ds_read_b128 v[176:179], %[va1] offset:4224\n\t"
      "v_exp_f32 v138, v138\n\t"
      "v_exp_f32 v139, v139\n\t"
      "v_exp_f32 v140, v140\n\t"
      "v_exp_f32 v141, v141\n\t"
      "v_exp_f32 v142, v142\n\t"
      "v_exp_f32 v143, v143\n\t"
      "v_exp_f32 v144, v144\n\t"
      "v_exp_f32 v145, v145\n\t"
      "v_smfmac_f32_32x32x32_bf16 %[oa], v[180:183], v[136:143], v228\n\t"
      "v_exp_f32 v146, v146\n\t"
      "v_exp_f32 v147, v147\n\t"
      "v_exp_f32 v148, v148\n\t"
      "v_mfma_f32_32x32x16_bf16 v[128:143], v[160:163], v[188:191], v[196:211]\n\t"
      "ds_read_b128 v[164:167], %[ka] offset:5120\n\t"
      "v_exp_f32 v149, v149\n\t"
      "v_exp_f32 v150, v150\n\t"
      "v_exp_f32 v151, v151\n\t"
      "v_exp_f32 v152, v152\n\t"
      "v_exp_f32 v153, v153\n\t"
      "v_smfmac_f32_32x32x32_bf16 %[ob], v[172:175], v[144:151], v228\n\t"
      "ds_read_b128 v[172:175], %[va0] offset:5280\n\t"
      "s_bitcmp1_b32 0x108, %[cnt]\n\t"
      "s_cbranch_scc1 9f\n\t"
      "7:\n\t"
      "v_exp_f32 v154, v154\n\t"
      "v_exp_f32 v155, v155\n\t"
      "v_exp_f32 v156, v156\n\t"
      "v_exp_f32 v157, v157\n\t"
      "v_exp_f32 v158, v158\n\t"
      "v_exp_f32 v159, v159\n\t"
      "v_exp_f32 v128, v128\n\t"
      "v_exp_f32 v129, v129\n\t"
      "v_smfmac_f32_32x32x32_bf16 %[ob], v[180:183], v[152:159], v228\n\t"
      "v_exp_f32 v130, v130\n\t"
      "v_exp_f32 v131, v131\n\t"
      "v_exp_f32 v132, v132\n\t"
      "v_mfma_f32_32x32x16_bf16 v[144:159], v[160:163], v[192:195], v[212:227]\n\t"
      "s_waitcnt lgkmcnt(1)\n\t"
      "v_exp_f32 v133, v133\n\t"
      "v_exp_f32 v134, v134\n\t"
      "v_exp_f32 v135, v135\n\t"
      "v_exp_f32 v136, v136\n\t"
      "v_exp_f32 v137, v137\n\t"
      "v_smfmac_f32_32x32x32_bf16 %[oa], v[168:171], v[128:135], v228\n\t"
      "ds_read_b128 v[180:183], %[va1] offset:5280\n\t"
      "v_exp_f32 v138, v138\n\t"
      "v_exp_f32 v139, v139\n\t"
      "v_exp_f32 v140, v140\n\t"
      "v_exp_f32 v141, v141\n\t"
      "v_exp_f32 v142, v142\n\t"
      "v_exp_f32 v143, v143\n\t"
      "v_exp_f32 v144, v144\n\t"
      "v_exp_f32 v145, v145\n\t"
      "v_smfmac_f32_32x32x32_bf16 %[oa], v[176:179], v[136:143], v228\n\t"
      "v_exp_f32 v146, v146\n\t"
      "v_exp_f32 v147, v147\n\t"
      "v_exp_f32 v148, v148\n\t"
      "v_mfma_f32_32x32x16_bf16 v[128:143], v[164:167], v[188:191], v[196:211]\n\t"
      "ds_read_b128 v[160:163], %[ka] offset:6144\n\t"
      "v_exp_f32 v149, v149\n\t"
      "v_exp_f32 v150, v150\n\t"
      "v_exp_f32 v151, v151\n\t"
      "v_exp_f32 v152, v152\n\t"
      "v_exp_f32 v153, v153\n\t"
      "v_smfmac_f32_32x32x32_bf16 %[ob], v[168:171], v[144:151], v228\n\t"
      "ds_read_b128 v[168:171], %[va0] offset:6336\n\t"
      "v_exp_f32 v154, v154\n\t"
      "v_exp_f32 v155, v155\n\t"
      "v_exp_f32 v156, v156\n\t"
      "v_exp_f32 v157, v157\n\t"
      "v_exp_f32 v158, v158\n\t"
      "v_exp_f32 v159, v159\n\t"
      "v_exp_f32 v128, v128\n\t"
      "v_exp_f32 v129, v129\n\t"
      "v_smfmac_f32_32x32x32_bf16 %[ob], v[176:179], v[152:159], v228\n\t"
      "v_exp_f32 v130, v130\n\t"
      "v_exp_f32 v131, v131\n\t"
      "v_exp_f32 v132, v132\n\t"
      "v_mfma_f32_32x32x16_bf16 v[144:159], v[164:167], v[192:195], v[212:227]\n\t"
      "s_waitcnt lgkmcnt(1)\n\t"
      "v_exp_f32 v133, v133\n\t"
      "v_exp_f32 v134, v134\n\t"
      "v_exp_f32 v135, v135\n\t"
      "v_exp_f32 v136, v136\n\t"
      "v_exp_f32 v137, v137\n\t"
      "v_smfmac_f32_32x32x32_bf16 %[oa], v[172:175], v[128:135], v228\n\t"
      "ds_read_b128 v[176:179], %[va1] offset:6336\n\t"
      "v_exp_f32 v138, v138\n\t"
      "v_exp_f32 v139, v139\n\t"
      "v_exp_f32 v140, v140\n\t"
      "v_exp_f32 v141, v141\n\t"
      "v_exp_f32 v142, v142\n\t"
      "v_exp_f32 v143, v143\n\t"
      "v_exp_f32 v144, v144\n\t"
      "v_exp_f32 v145, v145\n\t"
      "v_smfmac_f32_32x32x32_bf16 %[oa], v[180:183], v[136:143], v228\n\t"
      "v_exp_f32 v146, v146\n\t"
      "v_exp_f32 v147, v147\n\t"
      "v_exp_f32 v148, v148\n\t"
      "v_mfma_f32_32x32x16_bf16 v[128:143], v[160:163], v[188:191], v[196:211]\n\t"
      "ds_read_b128 v[164:167], %[ka] offset:7168\n\t"
      "v_exp_f32 v149, v149\n\t"
      "v_exp_f32 v150, v150\n\t"
      "v_exp_f32 v151, v151\n\t"
      "v_exp_f32 v152, v152\n\t"
      "v_exp_f32 v153, v153\n\t"
      "v_smfmac_f32_32x32x32_bf16 %[ob], v[172:175], v[144:151], v228\n\t"
      "ds_read_b128 v[172:175], %[va0] offset:7392\n\t"
      "v_exp_f32 v154, v154\n\t"
      "v_exp_f32 v155, v155\n\t"
      "v_exp_f32 v156, v156\n\t"
      "v_exp_f32 v157, v157\n\t"
      "v_exp_f32 v158, v158\n\t"
      "v_exp_f32 v159, v159\n\t"
      "v_exp_f32 v128, v128\n\t"
      "v_exp_f32 v129, v129\n\t"
      "v_smfmac_f32_32x32x32_bf16 %[ob], v[180:183], v[152:159], v228\n\t"
      "s_bitcmp1_b32 0x1a0, %[cnt]\n\t"
      "v_exp_f32 v130, v130\n\t"
      "v_exp_f32 v131, v131\n\t"
      "v_exp_f32 v132, v132\n\t"
      "v_mfma_f32_32x32x16_bf16 v[144:159], v[160:163], v[192:195], v[212:227]\n\t"
      "s_waitcnt lgkmcnt(1)\n\t"
      "v_exp_f32 v133, v133\n\t"
      "v_exp_f32 v134, v134\n\t"
      "v_exp_f32 v135, v135\n\t"
      "v_exp_f32 v136, v136\n\t"
      "v_exp_f32 v137, v137\n\t"
      "v_smfmac_f32_32x32x32_bf16 %[oa], v[168:171], v[128:135], v228\n\t"
      "ds_read_b128 v[180:183], %[va1] offset:7392\n\t"
      "v_exp_f32 v138, v138\n\t"
      "v_exp_f32 v139, v139\n\t"
      "v_exp_f32 v140, v140\n\t"
      "v_exp_f32 v141, v141\n\t"
      "v_exp_f32 v142, v142\n\t"
      "v_exp_f32 v143, v143\n\t"
      "v_exp_f32 v144, v144\n\t"
      "v_exp_f32 v145, v145\n\t"
      "v_smfmac_f32_32x32x32_bf16 %[oa], v[176:179], v[136:143], v228\n\t"
      "s_cbranch_scc1 2f\n\t"
      "3:\n\t"
      "v_exp_f32 v146, v146\n\t"
      "v_exp_f32 v147, v147\n\t"
      "v_exp_f32 v148, v148\n\t"
      "v_mfma_f32_32x32x16_bf16 v[128:143], v[164:167], v[188:191], v[196:211]\n\t"
      "ds_read_b128 v[160:163], %[ka] offset:16640\n\t"
      "v_exp_f32 v149, v149\n\t"
      "v_exp_f32 v150, v150\n\t"
      "v_exp_f32 v151, v151\n\t"
      "v_exp_f32 v152, v152\n\t"
      "v_exp_f32 v153, v153\n\t"
      "v_smfmac_f32_32x32x32_bf16 %[ob], v[168:171], v[144:151], v228\n\t"
      "ds_read_b128 v[168:171], %[va0] offset:16640\n\t"
      "v_exp_f32 v154, v154\n\t"
      "v_exp_f32 v155, v155\n\t"
      "v_exp_f32 v156, v156\n\t"
      "v_exp_f32 v157, v157\n\t"
      "v_exp_f32 v158, v158\n\t"
      "v_exp_f32 v159, v159\n\t"
      "v_exp_f32 v128, v128\n\t"
      "v_exp_f32 v129, v129\n\t"
      "v_smfmac_f32_32x32x32_bf16 %[ob], v[176:179], v[152:159], v228\n\t"
      "v_exp_f32 v130, v130\n\t"
      "v_exp_f32 v131, v131\n\t"
      "v_exp_f32 v132, v132\n\t"
      "v_mfma_f32_32x32x16_bf16 v[144:159], v[164:167], v[192:195], v[212:227]\n\t"
      "s_waitcnt lgkmcnt(1)\n\t"
      "v_exp_f32 v133, v133\n\t"
      "v_exp_f32 v134, v134\n\t"
      "v_exp_f32 v135, v135\n\t"
      "v_exp_f32 v136, v136\n\t"
      "v_exp_f32 v137, v137\n\t"
      "v_smfmac_f32_32x32x32_bf16 %[oa], v[172:175], v[128:135], v228\n\t"
      "ds_read_b128 v[176:179], %[va1] offset:16640\n\t"
      "v_exp_f32 v138, v138\n\t"
      "v_exp_f32 v139, v139\n\t"
      "v_exp_f32 v140, v140\n\t"
      "v_exp_f32 v141, v141\n\t"
      "v_exp_f32 v142, v142\n\t"
      "v_exp_f32 v143, v143\n\t"
      "v_exp_f32 v144, v144\n\t"
      "v_exp_f32 v145, v145\n\t"
      "v_smfmac_f32_32x32x32_bf16 %[oa], v[180:183], v[136:143], v228\n\t"
      "s_sub_u32 %[cnt], %[cnt], 1\n\t"
      "v_exp_f32 v146, v146\n\t"
      "v_exp_f32 v147, v147\n\t"
      "v_exp_f32 v148, v148\n\t"
      "v_mfma_f32_32x32x16_bf16 v[128:143], v[160:163], v[188:191], v[196:211]\n\t"
      "ds_read_b128 v[164:167], %[ka] offset:17664\n\t"
      "v_exp_f32 v149, v149\n\t"
      "v_exp_f32 v150, v150\n\t"
      "v_exp_f32 v151, v151\n\t"
      "v_exp_f32 v152, v152\n\t"
      "v_exp_f32 v153, v153\n\t"
      "v_smfmac_f32_32x32x32_bf16 %[ob], v[172:175], v[144:151], v228\n\t"
      "ds_read_b128 v[172:175], %[va0] offset:17696\n\t"
      "s_cmp_lg_u32 %[cnt], 0\n\t"
      "v_add_u32 %[ka], 16640, %[ka]\n\t"
      "v_add_u32 %[va0], 16640, %[va0]\n\t"
      "v_add_u32 %[va1], 16640, %[va1]\n\t"
      "v_exp_f32 v154, v154\n\t"
      "v_exp_f32 v155, v155\n\t"
      "v_exp_f32 v156, v156\n\t"
      "v_exp_f32 v157, v157\n\t"
      "v_exp_f32 v158, v158\n\t"
      "v_exp_f32 v159, v159\n\t"
      "s_cbranch_scc1 1b\n\t"
      "s_branch 4f\n\t"
      "9:\n\t"
      "s_waitcnt vmcnt(0)\n\t"
      "s_cmp_eq_u32 %[cnt], 8\n\t"
      "s_cbranch_scc1 13f\n\t"
      "s_mov_b64 exec, 1\n\t"
      "ds_add_u32 %[cb], v185 offset:20\n\t"
      "s_mov_b64 exec, -1\n\t"
      "s_branch 7b\n\t"
      "13:\n\t"
      "s_mov_b64 exec, 1\n\t"
      "ds_add_u32 %[cb], v185 offset:12\n\t"
      "s_mov_b64 exec, -1\n\t"
      "s_branch 7b\n\t"
      "2:\n\t"
      "5:\n\t"
      "ds_read_b32 v184, %[cb] offset:12\n\t"
      "s_waitcnt lgkmcnt(0)\n\t"
      "v_readfirstlane_b32 %[tmp], v184\n\t"
      "s_nop 3\n\t"
      "s_cmp_lt_u32 %[tmp], 4\n\t"
      "s_cbranch_scc1 5b\n\t"
      "ds_read_b32 %[g0], %[ga]\n\t"
      "ds_read_b32 %[g1], %[ga] offset:128\n\t"
      "v_add_u32 %[cb], 4, %[cb]\n\t"
      "s_branch 3b\n\t"
      "4:\n\t"
      "s_nop 0\n\t"
      "v_smfmac_f32_32x32x32_bf16 %[ob], v[180:183], v[152:159], v228\n\t"
      "s_waitcnt lgkmcnt(0)\n\t"
      "s_nop 11\n\t"
        : [oa] "+v"(o[0]), [ob] "+v"(o[1]), [ka] "+v"(ka), [va0] "+v"(va0), [va1] "+v"(va1), [cnt] "+s"(cnt), [tmp] "=&s"(tmp), [cb] "+v"(cbk), [cap] "+s"(cap), [g0] "=&v"(g[0]), [g1] "=&v"(g[1])
        : [tv] "v"(tagv), [tag] "s"(tag), [ra] "s"(wsr), [f1] "s"(F_1 + (cg * 64 + tt) * FS), [fo] "v"(fo), [l16] "v"(lane * 16), [l8] "v"(lane * 8), [ga] "v"(lds0 + L_G + (w * 64 + r) * 4),
          [mq] "s"(lds0 + L_Q + w * 2048), [mk] "s"(lds0 + rt * 2048), [mv] "s"(lds0 + 8192 + 2 * rt * VT), [sq] "s"(WS_Q + (hd * 64 + qt0) * 1024), [sk] "s"(ksrc), [t1] "s"((unsigned)t1), [oa1] "v"(lds0 + SLOT + 8192 + (2 * rt + ((lane >> 1) & 1)) * VT + 1024 + (lane & 1) * 16), [od] "v"(u32x4{1u, 1u, 1u, 1u} * ((lane & 1) ? 0u : 0x3f803f80u))
        : "v128", "v129", "v130", "v131", "v132", "v133", "v134", "v135", "v136", "v137", "v138", "v139", "v140", "v141", "v142", "v143", "v144", "v145", "v146", "v147", "v148", "v149", "v150", "v151", "v152", "v153", "v154", "v155", "v156", "v157", "v158", "v159", "v160", "v161", "v162", "v163", "v164", "v165", "v166", "v167", "v168", "v169", "v170", "v171", "v172", "v173", "v174", "v175", "v176", "v177", "v178", "v179", "v180", "v181", "v182", "v183", "v184", "v185", "v186", "v187", "v188", "v189", "v190", "v191", "v192", "v193", "v194", "v195", "v196", "v197", "v198", "v199", "v200", "v201", "v202", "v203", "v204", "v205", "v206", "v207", "v208", "v209", "v210", "v211", "v212", "v213", "v214", "v215", "v216", "v217", "v218", "v219", "v220", "v221", "v222", "v223", "v224", "v225", "v226", "v227", "v228", "s88", "s89", "scc", "vcc", "memory");
    }
    const unsigned od = WS_O + (qt0 * 32 + hd) * 1024 + lane * 16;
#pragma unroll
    for (int q = 0; q < 2; ++q) st16(wsr, od + q * 32768, pack8(o[q], 0, g[q] * __builtin_amdgcn_rcpf(o[q][8])));
    asm volatile("" ::: "memory");
#pragma unroll
    for (int q = 0; q < 2; ++q) {
      const float big = ((fabsf(o[q][0]) + fabsf(o[q][1])) + (fabsf(o[q][2]) + fabsf(o[q][3]))) + ((fabsf(o[q][4]) + fabsf(o[q][5])) + (fabsf(o[q][6]) + fabsf(o[q][7])));
      if (__builtin_expect(!__all(o[q][8] < 0x1p100f && big < 0x1p120f), 0)) {
        attn_slow(o[q], wsr, hd, qt0 + q, lane);
        drain();
        st16(wsr, od + q * 32768, pack8(o[q], 0, g[q] * __builtin_amdgcn_rcpf(o[q][8])));
      }
    }
    lds_inc<C_D>(cb, 1u);
    {
      unsigned sw, sa, cnt, cap = SPIN_CAP;
      const unsigned va0 = WS_O + ((2 * tb) * 32 + 8 * w) * 1024 + lane * 16;
      asm volatile(
      "s_mov_b64 exec, 0xff\n\t"
      "buffer_load_dwordx2 v[46:47], %[fo2], %[ra], 0 offen sc1\n\t"
      "s_mov_b64 exec, -1\n\t"
      "v_mov_b32 v43, 1\n\t"
      "s_mov_b32 %[sw], 0\n\t"
      "s_mov_b32 %[sa], -1\n\t"
      "s_setprio 0\n\t"
      "s_waitcnt vmcnt(1)\n\t"
      "s_mov_b64 exec, 0xffff\n\t"
      "buffer_store_dwordx2 %[tv], %[l8], %[ra], %[f3] offen sc1\n\t"
      "s_mov_b64 exec, -1\n\t"
      "s_waitcnt vmcnt(1)\n\t"
      "s_branch 30f\n\t"
      "29:\n\t"
      "36:\n\t"
      "ds_read_b32 v45, %[cb] offset:36\n\t"
      "s_waitcnt lgkmcnt(0)\n\t"
      "v_readfirstlane_b32 %[tmp], v45\n\t"
      "s_nop 3\n\t"
      "s_cmp_lt_u32 %[tmp], 4\n\t"
      "s_cbranch_scc1 36b\n\t"
      "ds_read_b64_tr_b16 v[192:193], %[t0] offset:0\n\t"
      "ds_read_b64_tr_b16 v[194:195], %[t0] offset:1024\n\t"
      "ds_read_b64_tr_b16 v[196:197], %[t1] offset:0\n\t"
      "ds_read_b64_tr_b16 v[198:199], %[t1] offset:1024\n\t"
      "ds_read_b64_tr_b16 v[200:201], %[t0] offset:2048\n\t"
      "ds_read_b64_tr_b16 v[202:203], %[t0] offset:3072\n\t"
      "ds_read_b64_tr_b16 v[204:205], %[t1] offset:2048\n\t"
      "ds_read_b64_tr_b16 v[206:207], %[t1] offset:3072\n\t"
      "ds_read_b64_tr_b16 v[208:209], %[t0] offset:4096\n\t"
      "ds_read_b64_tr_b16 v[210:211], %[t0] offset:5120\n\t"
      "ds_read_b64_tr_b16 v[212:213], %[t1] offset:4096\n\t"
      "ds_read_b64_tr_b16 v[214:215], %[t1] offset:5120\n\t"
      "ds_read_b64_tr_b16 v[216:217], %[t0] offset:6144\n\t"
      "ds_read_b64_tr_b16 v[218:219], %[t0] offset:7168\n\t"
      "ds_read_b64_tr_b16 v[220:221], %[t1] offset:6144\n\t"
      "ds_read_b64_tr_b16 v[222:223], %[t1] offset:7168\n\t"
      "ds_read_b64_tr_b16 v[224:225], %[t0] offset:8192\n\t"
      "ds_read_b64_tr_b16 v[226:227], %[t0] offset:9216\n\t"
      "ds_read_b64_tr_b16 v[228:229], %[t1] offset:8192\n\t"
      "ds_read_b64_tr_b16 v[230:231], %[t1] offset:9216\n\t"
      "ds_read_b64_tr_b16 v[232:233], %[t0] offset:10240\n\t"
      "ds_read_b64_tr_b16 v[234:235], %[t0] offset:11264\n\t"
      "ds_read_b64_tr_b16 v[236:237], %[t1] offset:10240\n\t"
      "ds_read_b64_tr_b16 v[238:239], %[t1] offset:11264\n\t"
      "ds_read_b64_tr_b16 v[240:241], %[t0] offset:12288\n\t"
      "ds_read_b64_tr_b16 v[242:243], %[t0] offset:13312\n\t"
      "ds_read_b64_tr_b16 v[244:245], %[t1] offset:12288\n\t"
      "ds_read_b64_tr_b16 v[246:247], %[t1] offset:13312\n\t"
      "ds_read_b64_tr_b16 v[248:249], %[t0] offset:14336\n\t"
      "ds_read_b64_tr_b16 v[250:251], %[t0] offset:15360\n\t"
      "ds_read_b64_tr_b16 v[252:253], %[t1] offset:14336\n\t"
      "ds_read_b64_tr_b16 v[254:255], %[t1] offset:15360\n\t"
      "s_mov_b32 %[sa], 0\n\t"
      "s_or_b32 %[sw], %[sw], %[cnt]\n\t"
      "s_waitcnt lgkmcnt(0)\n\t"
      "31:\n\t"
      "s_cmp_eq_u32 %[sw], 0xff\n\t"
      "s_cbranch_scc1 32f\n\t"
      "s_mov_b64 exec, 0xff\n\t"
      "buffer_load_dwordx2 v[46:47], %[fo2], %[ra], 0 offen sc1\n\t"
      "s_mov_b64 exec, -1\n\t"
      "32:\n\t"
      "s_waitcnt vmcnt(0)\n\t"
      "30:\n\t"
      "v_cmp_eq_u64 vcc, %[tag], v[46:47]\n\t"
      "s_sub_u32 %[cap], %[cap], 1\n\t"
      "s_and_b32 %[cnt], vcc_lo, 0xff\n\t"
      "s_cmp_lt_i32 %[cap], 1\n\t"
      "s_cselect_b32 %[cnt], 0xff, %[cnt]\n\t"
      "s_andn2_b32 %[cnt], %[cnt], %[sw]\n\t"
      "s_cbranch_scc1 33f\n\t"
      "s_cmp_lt_i32 %[sa], 0\n\t"
      "s_cbranch_scc1 29b\n\t"
      "s_bitcmp1_b32 %[sw], %[sa]\n\t"
      "s_cbranch_scc1 34f\n\t"
      "s_sleep 10\n\t"
      "s_branch 31b\n\t"
      "33:\n\t"
      "s_bitcmp1_b32 %[cnt], 0\n\t"
      "s_cbranch_scc0 10f\n\t"
      "s_mov_b32 %[tmp], 0\n\t"
      "buffer_load_dwordx4 v[128:131], %[va0], %[ra], %[tmp] offen sc1\n\t"
      "buffer_load_dwordx4 v[132:135], %[va1], %[ra], %[tmp] offen sc1\n\t"
      "10:\n\t"
      "s_bitcmp1_b32 %[cnt], 1\n\t"
      "s_cbranch_scc0 11f\n\t"
      "s_mov_b32 %[tmp], 1024\n\t"
      "buffer_load_dwordx4 v[136:139], %[va0], %[ra], %[tmp] offen sc1\n\t"
      "buffer_load_dwordx4 v[140:143], %[va1], %[ra], %[tmp] offen sc1\n\t"
      "11:\n\t"
      "s_bitcmp1_b32 %[cnt], 2\n\t"
      "s_cbranch_scc0 12f\n\t"
      "s_mov_b32 %[tmp], 2048\n\t"
      "buffer_load_dwordx4 v[144:147], %[va0], %[ra], %[tmp] offen sc1\n\t"
      "buffer_load_dwordx4 v[148:151], %[va1], %[ra], %[tmp] offen sc1\n\t"
      "12:\n\t"
      "s_bitcmp1_b32 %[cnt], 3\n\t"
      "s_cbranch_scc0 13f\n\t"
      "s_mov_b32 %[tmp], 3072\n\t"
      "buffer_load_dwordx4 v[152:155], %[va0], %[ra], %[tmp] offen sc1\n\t"
      "buffer_load_dwordx4 v[156:159], %[va1], %[ra], %[tmp] offen sc1\n\t"
      "13:\n\t"
      "s_bitcmp1_b32 %[cnt], 4\n\t"
      "s_cbranch_scc0 14f\n\t"
      "s_mov_b32 %[tmp], 4096\n\t"
      "buffer_load_dwordx4 v[160:163], %[va0], %[ra], %[tmp] offen sc1\n\t"
      "buffer_load_dwordx4 v[164:167], %[va1], %[ra], %[tmp] offen sc1\n\t"
      "14:\n\t"
      "s_bitcmp1_b32 %[cnt], 5\n\t"
      "s_cbranch_scc0 15f\n\t"
      "s_mov_b32 %[tmp], 5120\n\t"
      "buffer_load_dwordx4 v[168:171], %[va0], %[ra], %[tmp] offen sc1\n\t"
      "buffer_load_dwordx4 v[172:175], %[va1], %[ra], %[tmp] offen sc1\n\t"
      "15:\n\t"
      "s_bitcmp1_b32 %[cnt], 6\n\t"
      "s_cbranch_scc0 16f\n\t"
      "s_mov_b32 %[tmp], 6144\n\t"
      "buffer_load_dwordx4 v[176:179], %[va0], %[ra], %[tmp] offen sc1\n\t"
      "buffer_load_dwordx4 v[180:183], %[va1], %[ra], %[tmp] offen sc1\n\t"
      "16:\n\t"
      "s_bitcmp1_b32 %[cnt], 7\n\t"
      "s_cbranch_scc0 17f\n\t"
      "s_mov_b32 %[tmp], 7168\n\t"
      "buffer_load_dwordx4 v[184:187], %[va0], %[ra], %[tmp] offen sc1\n\t"
      "buffer_load_dwordx4 v[188:191], %[va1], %[ra], %[tmp] offen sc1\n\t"
      "17:\n\t"
      "s_cmp_lt_i32 %[sa], 0\n\t"
      "s_cbranch_scc1 29b\n\t"
      "34:\n\t"
      "s_cmp_lg_u32 %[sa], 0\n\t"
      "s_cbranch_scc1 20f\n\t"
      "s_bitcmp1_b32 %[sw], 0\n\t"
      "s_cbranch_scc0 35f\n\t"
      "v_mfma_f32_32x32x16_bf16 v[64:79], v[192:195], v[128:131], 0\n\t"
      "v_mfma_f32_32x32x16_bf16 v[80:95], v[192:195], v[132:135], 0\n\t"
      "v_mfma_f32_32x32x16_bf16 v[96:111], v[196:199], v[128:131], 0\n\t"
      "v_mfma_f32_32x32x16_bf16 v[112:127], v[196:199], v[132:135], 0\n\t"
      "s_mov_b32 %[sa], 1\n\t"
      "20:\n\t"
      "s_cmp_lg_u32 %[sa], 1\n\t"
      "s_cbranch_scc1 21f\n\t"
      "s_bitcmp1_b32 %[sw], 1\n\t"
      "s_cbranch_scc0 35f\n\t"
      "v_mfma_f32_32x32x16_bf16 v[64:79], v[200:203], v[136:139], v[64:79]\n\t"
      "v_mfma_f32_32x32x16_bf16 v[80:95], v[200:203], v[140:143], v[80:95]\n\t"
      "v_mfma_f32_32x32x16_bf16 v[96:111], v[204:207], v[136:139], v[96:111]\n\t"
      "v_mfma_f32_32x32x16_bf16 v[112:127], v[204:207], v[140:143], v[112:127]\n\t"
      "s_mov_b32 %[sa], 2\n\t"
      "21:\n\t"
      "s_cmp_lg_u32 %[sa], 2\n\t"
      "s_cbranch_scc1 22f\n\t"
      "s_bitcmp1_b32 %[sw], 2\n\t"
      "s_cbranch_scc0 35f\n\t"
      "v_mfma_f32_32x32x16_bf16 v[64:79], v[208:211], v[144:147], v[64:79]\n\t"
      "v_mfma_f32_32x32x16_bf16 v[80:95], v[208:211], v[148:151], v[80:95]\n\t"
      "v_mfma_f32_32x32x16_bf16 v[96:111], v[212:215], v[144:147], v[96:111]\n\t"
      "v_mfma_f32_32x32x16_bf16 v[112:127], v[212:215], v[148:151], v[112:127]\n\t"
      "s_mov_b32 %[sa], 3\n\t"
      "22:\n\t"
      "s_cmp_lg_u32 %[sa], 3\n\t"
      "s_cbranch_scc1 23f\n\t"
      "s_bitcmp1_b32 %[sw], 3\n\t"
      "s_cbranch_scc0 35f\n\t"
      "v_mfma_f32_32x32x16_bf16 v[64:79], v[216:219], v[152:155], v[64:79]\n\t"
      "v_mfma_f32_32x32x16_bf16 v[80:95], v[216:219], v[156:159], v[80:95]\n\t"
      "v_mfma_f32_32x32x16_bf16 v[96:111], v[220:223], v[152:155], v[96:111]\n\t"
      "v_mfma_f32_32x32x16_bf16 v[112:127], v[220:223], v[156:159], v[112:127]\n\t"
      "s_mov_b32 %[sa], 4\n\t"
      "23:\n\t"
      "s_cmp_lg_u32 %[sa], 4\n\t"
      "s_cbranch_scc1 24f\n\t"
      "s_bitcmp1_b32 %[sw], 4\n\t"
      "s_cbranch_scc0 35f\n\t"
      "v_mfma_f32_32x32x16_bf16 v[64:79], v[224:227], v[160:163], v[64:79]\n\t"
      "v_mfma_f32_32x32x16_bf16 v[80:95], v[224:227], v[164:167], v[80:95]\n\t"
      "v_mfma_f32_32x32x16_bf16 v[96:111], v[228:231], v[160:163], v[96:111]\n\t"
      "v_mfma_f32_32x32x16_bf16 v[112:127], v[228:231], v[164:167], v[112:127]\n\t"
      "s_mov_b32 %[sa], 5\n\t"
      "24:\n\t"
      "s_cmp_lg_u32 %[sa], 5\n\t"
      "s_cbranch_scc1 25f\n\t"
      "s_bitcmp1_b32 %[sw], 5\n\t"
      "s_cbranch_scc0 35f\n\t"
      "v_mfma_f32_32x32x16_bf16 v[64:79], v[232:235], v[168:171], v[64:79]\n\t"
      "v_mfma_f32_32x32x16_bf16 v[80:95], v[232:235], v[172:175], v[80:95]\n\t"
      "v_mfma_f32_32x32x16_bf16 v[96:111], v[236:239], v[168:171], v[96:111]\n\t"
      "v_mfma_f32_32x32x16_bf16 v[112:127], v[236:239], v[172:175], v[112:127]\n\t"
      "s_mov_b32 %[sa], 6\n\t"
      "25:\n\t"
      "s_cmp_lg_u32 %[sa], 6\n\t"
      "s_cbranch_scc1 26f\n\t"
      "s_bitcmp1_b32 %[sw], 6\n\t"
      "s_cbranch_scc0 35f\n\t"
      "v_mfma_f32_32x32x16_bf16 v[64:79], v[240:243], v[176:179], v[64:79]\n\t"
      "v_mfma_f32_32x32x16_bf16 v[80:95], v[240:243], v[180:183], v[80:95]\n\t"
      "v_mfma_f32_32x32x16_bf16 v[96:111], v[244:247], v[176:179], v[96:111]\n\t"
      "v_mfma_f32_32x32x16_bf16 v[112:127], v[244:247], v[180:183], v[112:127]\n\t"
      "s_mov_b32 %[sa], 7\n\t"
      "26:\n\t"
      "s_cmp_lg_u32 %[sa], 7\n\t"
      "s_cbranch_scc1 27f\n\t"
      "s_bitcmp1_b32 %[sw], 7\n\t"
      "s_cbranch_scc0 35f\n\t"
      "v_mfma_f32_32x32x16_bf16 v[64:79], v[248:251], v[184:187], v[64:79]\n\t"
      "v_mfma_f32_32x32x16_bf16 v[80:95], v[248:251], v[188:191], v[80:95]\n\t"
      "v_mfma_f32_32x32x16_bf16 v[96:111], v[252:255], v[184:187], v[96:111]\n\t"
      "v_mfma_f32_32x32x16_bf16 v[112:127], v[252:255], v[188:191], v[112:127]\n\t"
      "s_mov_b32 %[sa], 8\n\t"
      "27:\n\t"
      "35:\n\t"
      "s_or_b32 %[sw], %[sw], %[cnt]\n\t"
      "s_cmp_lg_u32 %[sa], 8\n\t"
      "s_cbranch_scc1 31b\n\t"
      "s_nop 7\n\t"
      "ds_write_b128 %[rwr], v[64:67] offset:0\n\t"
      "ds_write_b128 %[rwr], v[68:71] offset:32\n\t"
      "ds_write_b128 %[rwr], v[72:75] offset:64\n\t"
      "ds_write_b128 %[rwr], v[76:79] offset:96\n\t"
      "ds_write_b128 %[rwr], v[80:83] offset:8704\n\t"
      "ds_write_b128 %[rwr], v[84:87] offset:8736\n\t"
      "ds_write_b128 %[rwr], v[88:91] offset:8768\n\t"
      "ds_write_b128 %[rwr], v[92:95] offset:8800\n\t"
      "ds_write_b128 %[rwr], v[96:99] offset:128\n\t"
      "ds_write_b128 %[rwr], v[100:103] offset:160\n\t"
      "ds_write_b128 %[rwr], v[104:107] offset:192\n\t"
      "ds_write_b128 %[rwr], v[108:111] offset:224\n\t"
      "ds_write_b128 %[rwr], v[112:115] offset:8832\n\t"
      "ds_write_b128 %[rwr], v[116:119] offset:8864\n\t"
      "ds_write_b128 %[rwr], v[120:123] offset:8896\n\t"
      "ds_write_b128 %[rwr], v[124:127] offset:8928\n\t"
      "v_add_u32 v47, 69632, %[rrd]\n\t"
      "s_waitcnt lgkmcnt(0)\n\t"
      "s_mov_b64 exec, 1\n\t"
      "ds_add_u32 %[cb], v43 offset:28\n\t"
      "s_mov_b64 exec, -1\n\t"
      "37:\n\t"
      "ds_read_b32 v46, %[cb] offset:28\n\t"
      "s_waitcnt lgkmcnt(0)\n\t"
      "v_readfirstlane_b32 %[tmp], v46\n\t"
      "s_nop 3\n\t"
      "s_cmp_lt_u32 %[tmp], 8\n\t"
      "s_cbranch_scc1 37b\n\t"
      "ds_read_b128 v[128:131], %[rrd] offset:0\n\t"
      "ds_read_b128 v[132:135], %[rrd] offset:16\n\t"
      "ds_read_b128 v[136:139], %[rrd] offset:17408\n\t"
      "ds_read_b128 v[140:143], %[rrd] offset:17424\n\t"
      "ds_read_b128 v[144:147], %[rrd] offset:34816\n\t"
      "ds_read_b128 v[148:151], %[rrd] offset:34832\n\t"
      "ds_read_b128 v[152:155], %[rrd] offset:52224\n\t"
      "ds_read_b128 v[156:159], %[rrd] offset:52240\n\t"
      "ds_read_b128 v[160:163], v47 offset:0\n\t"
      "ds_read_b128 v[164:167], v47 offset:16\n\t"
      "ds_read_b128 v[168:171], v47 offset:17408\n\t"
      "ds_read_b128 v[172:175], v47 offset:17424\n\t"
      "ds_read_b128 v[176:179], v47 offset:34816\n\t"
      "ds_read_b128 v[180:183], v47 offset:34832\n\t"
      "ds_read_b128 v[184:187], v47 offset:52224\n\t"
      "ds_read_b128 v[188:191], v47 offset:52240\n\t"
      "s_waitcnt lgkmcnt(12)\n\t"
      "v_pk_add_f32 v[128:129], v[128:129], v[136:137]\n\t"
      "v_pk_add_f32 v[130:131], v[130:131], v[138:139]\n\t"
      "v_pk_add_f32 v[132:133], v[132:133], v[140:141]\n\t"
      "v_pk_add_f32 v[134:135], v[134:135], v[142:143]\n\t"
      "s_waitcnt lgkmcnt(8)\n\t"
      "v_pk_add_f32 v[144:145], v[144:145], v[152:153]\n\t"
      "v_pk_add_f32 v[146:147], v[146:147], v[154:155]\n\t"
      "v_pk_add_f32 v[148:149], v[148:149], v[156:157]\n\t"
      "v_pk_add_f32 v[150:151], v[150:151], v[158:159]\n\t"
      "v_pk_add_f32 v[128:129], v[128:129], v[144:145]\n\t"
      "v_pk_add_f32 v[130:131], v[130:131], v[146:147]\n\t"
      "v_pk_add_f32 v[132:133], v[132:133], v[148:149]\n\t"
      "v_pk_add_f32 v[134:135], v[134:135], v[150:151]\n\t"
      "s_waitcnt lgkmcnt(4)\n\t"
      "v_pk_add_f32 v[160:161], v[160:161], v[168:169]\n\t"
      "v_pk_add_f32 v[162:163], v[162:163], v[170:171]\n\t"
      "v_pk_add_f32 v[164:165], v[164:165], v[172:173]\n\t"
      "v_pk_add_f32 v[166:167], v[166:167], v[174:175]\n\t"
      "v_pk_add_f32 v[128:129], v[128:129], v[160:161]\n\t"
      "v_pk_add_f32 v[130:131], v[130:131], v[162:163]\n\t"
      "v_pk_add_f32 v[132:133], v[132:133], v[164:165]\n\t"
      "v_pk_add_f32 v[134:135], v[134:135], v[166:167]\n\t"
      "s_waitcnt lgkmcnt(0)\n\t"
      "v_pk_add_f32 v[176:177], v[176:177], v[184:185]\n\t"
      "v_pk_add_f32 v[178:179], v[178:179], v[186:187]\n\t"
      "v_pk_add_f32 v[180:181], v[180:181], v[188:189]\n\t"
      "v_pk_add_f32 v[182:183], v[182:183], v[190:191]\n\t"
      "v_pk_add_f32 v[128:129], v[128:129], v[176:177]\n\t"
      "v_pk_add_f32 v[130:131], v[130:131], v[178:179]\n\t"
      "v_pk_add_f32 v[132:133], v[132:133], v[180:181]\n\t"
      "v_pk_add_f32 v[134:135], v[134:135], v[182:183]\n\t"
      "v_cvt_pk_bf16_f32 v136, v128, v129\n\t"
      "v_cvt_pk_bf16_f32 v137, v130, v131\n\t"
      "v_cvt_pk_bf16_f32 v138, v132, v133\n\t"
      "v_cvt_pk_bf16_f32 v139, v134, v135\n\t"
      "buffer_store_dwordx4 v[136:139], %[vo], %[rout], 0 offen sc1\n\t"
      "s_nop 1\n\t"
        : [sw] "=&s"(sw), [sa] "=&s"(sa), [cnt] "=&s"(cnt), [cap] "+s"(cap), [tmp] "=&s"(tmp)
        : OUT_OPERANDS(lds0 + w * 64 * RED_STRIDE), [va0] "v"(va0), [va1] "v"(va0 + 32 * 1024), [fo2] "v"(F_O + (tb * 32 + 8 * w + (lane & 7)) * FS),
          [tv] "v"(tagv), [l8] "v"(lane * 8), [f3] "s"(F_O + ((qb * 4 + w) * 32 + hd) * FS)
        : "v43", "v44", "v45", "v46", "v47", "v48", "v49", "v50", "v51", "v52", "v53", "v54", "v55", "v56", "v57", "v58", "v59", "v60", "v61", "v62", "v63", "v64", "v65", "v66", "v67", "v68", "v69", "v70", "v71", "v72", "v73", "v74", "v75", "v76", "v77", "v78", "v79", "v80", "v81", "v82", "v83", "v84", "v85", "v86", "v87", "v88", "v89", "v90", "v91", "v92", "v93", "v94", "v95", "v96", "v97", "v98", "v99", "v100", "v101", "v102", "v103", "v104", "v105", "v106", "v107", "v108", "v109", "v110", "v111", "v112", "v113", "v114", "v115", "v116", "v117", "v118", "v119", "v120", "v121", "v122", "v123", "v124", "v125", "v126", "v127", "v128", "v129", "v130", "v131", "v132", "v133", "v134", "v135", "v136", "v137", "v138", "v139", "v140", "v141", "v142", "v143", "v144", "v145", "v146", "v147", "v148", "v149", "v150", "v151", "v152", "v153", "v154", "v155", "v156", "v157", "v158", "v159", "v160", "v161", "v162", "v163", "v164", "v165", "v166", "v167", "v168", "v169", "v170", "v171", "v172", "v173", "v174", "v175", "v176", "v177", "v178", "v179", "v180", "v181", "v182", "v183", "v184", "v185", "v186", "v187", "v188", "v189", "v190", "v191", "v192", "v193", "v194", "v195", "v196", "v197", "v198", "v199", "v200", "v201", "v202", "v203", "v204", "v205", "v206", "v207", "v208", "v209", "v210", "v211", "v212", "v213", "v214", "v215", "v216", "v217", "v218", "v219", "v220", "v221", "v222", "v223", "v224", "v225", "v226", "v227", "v228", "v229", "v230", "v231", "v232", "v233", "v234", "v235", "v236", "v237", "v238", "v239", "v240", "v241", "v242", "v243", "v244", "v245", "v246", "v247", "v248", "v249", "v250", "v251", "v252", "v253", "v254", "v255", "scc", "vcc", "memory");
    }
    return;
  }
  const int f = rt;
  {
    unsigned tm2; f32x16 a[4]; float gl; unsigned tmp;
    { const int lane = fresh_lane(), r = lane & 31, h = lane >> 5;
    const unsigned c16 = ((lane & 15) ^ ((lane >> 4) << 2)) * 16;
    const int woffs = rt * 16 * 3072 + cg * 192, foffs = rt * 16 * (DFF * 2) + (job2 < 0 ? 0 : job2) * 256;
    if (w == 4 && job2 == -2) dma1(lane * 16, gw, lds0 + L_GW);
    asm volatile(
      "v_mov_b32 v80, %[xv]\n\t"
      "v_xor_b32 v81, 64, %[xv]\n\t"
      "v_add_u32 v81, 7168, v81\n\t"
      "v_add_u32 v82, 14336, v80\n\t"
      "v_add_u32 v83, 14336, v81\n\t"
      "s_add_u32 %[tmp], %[r0], 0\n\t"
      "s_and_b32 %[tmp], %[tmp], 7\n\t"
      "s_mul_i32 %[tmp], %[tmp], 128\n\t"
      "s_add_u32 m0, %[dst], 33280\n\t"
      "s_nop 0\n\t"
      "buffer_load_dwordx4 v80, %[rx], %[tmp] offen offset:0 lds\n\t"
      "buffer_load_dwordx4 v81, %[rx], %[tmp] offen offset:1024 lds\n\t"
      "buffer_load_dwordx4 v82, %[rx], %[tmp] offen offset:2048 lds\n\t"
      "buffer_load_dwordx4 v83, %[rx], %[tmp] offen offset:3072 lds\n\t"
      "v_xor_b32 v84, 0, %[qv]\n\t"
      "v_add_u32 v84, 0, v84\n\t"
      "v_xor_b32 v85, 16, %[qv]\n\t"
      "v_add_u32 v85, 11264, v85\n\t"
      "v_xor_b32 v86, 32, %[qv]\n\t"
      "v_add_u32 v86, 22528, v86\n\t"
      "v_xor_b32 v87, 48, %[qv]\n\t"
      "v_add_u32 v87, 33792, v87\n\t"
      "s_add_u32 %[tmp], %[r0], 0\n\t"
      "s_and_b32 %[tmp], %[tmp], 7\n\t"
      "s_mul_i32 %[tmp], %[tmp], 196608\n\t"
      "s_add_u32 m0, %[dst], 99840\n\t"
      "s_nop 0\n\t"
      "s_mov_b32 exec_lo, 0xf0ff0fff\n\t"
      "s_mov_b32 exec_hi, 0xfff0ff0f\n\t"
      "buffer_load_dwordx4 v84, %[rq], %[tmp] offen offset:0 lds\n\t"
      "buffer_load_dwordx4 v85, %[rq], %[tmp] offen offset:1024 lds\n\t"
      "buffer_load_dwordx4 v86, %[rq], %[tmp] offen offset:2048 lds\n\t"
      "buffer_load_dwordx4 v87, %[rq], %[tmp] offen offset:3072 lds\n\t"
      "s_mov_b64 exec, -1\n\t"
      "s_add_u32 %[tmp], %[r0], 1\n\t"
      "s_and_b32 %[tmp], %[tmp], 7\n\t"
      "s_mul_i32 %[tmp], %[tmp], 128\n\t"
      "s_add_u32 m0, %[dst], 49920\n\t"
      "s_nop 0\n\t"
      "buffer_load_dwordx4 v80, %[rx], %[tmp] offen offset:0 lds\n\t"
      "buffer_load_dwordx4 v81, %[rx], %[tmp] offen offset:1024 lds\n\t"
      "buffer_load_dwordx4 v82, %[rx], %[tmp] offen offset:2048 lds\n\t"
      "buffer_load_dwordx4 v83, %[rx], %[tmp] offen offset:3072 lds\n\t"
      "s_add_u32 %[tmp], %[r0], 1\n\t"
      "s_and_b32 %[tmp], %[tmp], 7\n\t"
      "s_mul_i32 %[tmp], %[tmp], 196608\n\t"
      "s_add_u32 m0, %[dst], 116480\n\t"
      "s_nop 0\n\t"
      "s_mov_b32 exec_lo, 0xf0ff0fff\n\t"
      "s_mov_b32 exec_hi, 0xfff0ff0f\n\t"
      "buffer_load_dwordx4 v84, %[rq], %[tmp] offen offset:0 lds\n\t"
      "buffer_load_dwordx4 v85, %[rq], %[tmp] offen offset:1024 lds\n\t"
      "buffer_load_dwordx4 v86, %[rq], %[tmp] offen offset:2048 lds\n\t"
      "buffer_load_dwordx4 v87, %[rq], %[tmp] offen offset:3072 lds\n\t"
      "s_mov_b64 exec, -1\n\t"
      "v_xor_b32 v88, 0, %[fv]\n\t"
      "v_add_u32 v88, 0, v88\n\t"
      "v_xor_b32 v89, 16, %[fv]\n\t"
      "v_add_u32 v89, 13312, v89\n\t"
      "v_xor_b32 v90, 32, %[fv]\n\t"
      "v_add_u32 v90, 26624, v90\n\t"
      "v_xor_b32 v91, 48, %[fv]\n\t"
      "v_add_u32 v91, 39936, v91\n\t"
      "v_xor_b32 v92, 0, %[xo]\n\t"
      "v_add_u32 v92, %[bl], v92\n\t"
      "v_xor_b32 v93, 32, %[xo]\n\t"
      "v_add_u32 v93, %[bl], v93\n\t"
      "v_xor_b32 v94, 64, %[xo]\n\t"
      "v_add_u32 v94, %[bl], v94\n\t"
      "v_xor_b32 v95, 96, %[xo]\n\t"
      "v_add_u32 v95, %[bl], v95\n\t"
      "v_mov_b32 v79, 1\n\t"
      "s_load_dwordx2 %[tag], %[ep], 0x0\n\t"
      "s_waitcnt vmcnt(8) lgkmcnt(0)\n\t"
      "s_barrier\n\t"
      "s_add_u32 %[tmp], %[r0], 2\n\t"
      "s_and_b32 %[tmp], %[tmp], 7\n\t"
      "s_mul_i32 %[tmp], %[tmp], 128\n\t"
      "s_add_u32 m0, %[dst], 66560\n\t"
      "s_nop 0\n\t"
      "buffer_load_dwordx4 v80, %[rx], %[tmp] offen offset:0 lds\n\t"
      "buffer_load_dwordx4 v81, %[rx], %[tmp] offen offset:1024 lds\n\t"
      "buffer_load_dwordx4 v82, %[rx], %[tmp] offen offset:2048 lds\n\t"
      "buffer_load_dwordx4 v83, %[rx], %[tmp] offen offset:3072 lds\n\t"
      "ds_read_b128 v[128:131], v92 offset:0\n\t"
      "ds_read_b128 v[132:135], v93 offset:0\n\t"
      "ds_read_b128 v[136:139], v94 offset:0\n\t"
      "ds_read_b128 v[140:143], v95 offset:0\n\t"
      "s_add_u32 %[tmp], %[r0], 2\n\t"
      "s_and_b32 %[tmp], %[tmp], 7\n\t"
      "s_mul_i32 %[tmp], %[tmp], 196608\n\t"
      "s_add_u32 m0, %[dst], 133120\n\t"
      "s_nop 0\n\t"
      "s_mov_b32 exec_lo, 0xf0ff0fff\n\t"
      "s_mov_b32 exec_hi, 0xfff0ff0f\n\t"
      "buffer_load_dwordx4 v84, %[rq], %[tmp] offen offset:0 lds\n\t"
      "buffer_load_dwordx4 v85, %[rq], %[tmp] offen offset:1024 lds\n\t"
      "buffer_load_dwordx4 v86, %[rq], %[tmp] offen offset:2048 lds\n\t"
      "buffer_load_dwordx4 v87, %[rq], %[tmp] offen offset:3072 lds\n\t"
      "s_mov_b64 exec, -1\n\t"
      "s_waitcnt vmcnt(8) lgkmcnt(0)\n\t"
      "s_barrier\n\t"
      "s_add_u32 %[tmp], %[r0], 3\n\t"
      "s_and_b32 %[tmp], %[tmp], 7\n\t"
      "s_mul_i32 %[tmp], %[tmp], 128\n\t"
      "s_add_u32 m0, %[dst], 33280\n\t"
      "s_nop 0\n\t"
      "buffer_load_dwordx4 v80, %[rx], %[tmp] offen offset:0 lds\n\t"
      "buffer_load_dwordx4 v81, %[rx], %[tmp] offen offset:1024 lds\n\t"
      "buffer_load_dwordx4 v82, %[rx], %[tmp] offen offset:2048 lds\n\t"
      "buffer_load_dwordx4 v83, %[rx], %[tmp] offen offset:3072 lds\n\t"
      "ds_read_b128 v[144:147], v92 offset:16640\n\t"
      "ds_read_b128 v[148:151], v93 offset:16640\n\t"
      "ds_read_b128 v[152:155], v94 offset:16640\n\t"
      "ds_read_b128 v[156:159], v95 offset:16640\n\t"
      "s_add_u32 %[tmp], %[r0], 3\n\t"
      "s_and_b32 %[tmp], %[tmp], 7\n\t"
      "s_mul_i32 %[tmp], %[tmp], 196608\n\t"
      "s_add_u32 m0, %[dst], 99840\n\t"
      "s_nop 0\n\t"
      "s_mov_b32 exec_lo, 0xf0ff0fff\n\t"
      "s_mov_b32 exec_hi, 0xfff0ff0f\n\t"
      "buffer_load_dwordx4 v84, %[rq], %[tmp] offen offset:0 lds\n\t"
      "buffer_load_dwordx4 v85, %[rq], %[tmp] offen offset:1024 lds\n\t"
      "buffer_load_dwordx4 v86, %[rq], %[tmp] offen offset:2048 lds\n\t"
      "buffer_load_dwordx4 v87, %[rq], %[tmp] offen offset:3072 lds\n\t"
      "s_mov_b64 exec, -1\n\t"
      "s_waitcnt vmcnt(8) lgkmcnt(0)\n\t"
      "s_barrier\n\t"
      "s_add_u32 %[tmp], %[r0], 4\n\t"
      "s_and_b32 %[tmp], %[tmp], 7\n\t"
      "s_mul_i32 %[tmp], %[tmp], 128\n\t"
      "s_add_u32 m0, %[dst], 49920\n\t"
      "s_nop 0\n\t"
      "buffer_load_dwordx4 v80, %[rx], %[tmp] offen offset:0 lds\n\t"
      "buffer_load_dwordx4 v81, %[rx], %[tmp] offen offset:1024 lds\n\t"
      "buffer_load_dwordx4 v82, %[rx], %[tmp] offen offset:2048 lds\n\t"
      "buffer_load_dwordx4 v83, %[rx], %[tmp] offen offset:3072 lds\n\t"
      "ds_read_b128 v[160:163], v92 offset:33280\n\t"
      "ds_read_b128 v[164:167], v93 offset:33280\n\t"
      "ds_read_b128 v[168:171], v94 offset:33280\n\t"
      "ds_read_b128 v[172:175], v95 offset:33280\n\t"
      "s_add_u32 %[tmp], %[r0], 4\n\t"
      "s_and_b32 %[tmp], %[tmp], 7\n\t"
      "s_mul_i32 %[tmp], %[tmp], 196608\n\t"
      "s_add_u32 m0, %[dst], 116480\n\t"
      "s_nop 0\n\t"
      "s_mov_b32 exec_lo, 0xf0ff0fff\n\t"
      "s_mov_b32 exec_hi, 0xfff0ff0f\n\t"
      "buffer_load_dwordx4 v84, %[rq], %[tmp] offen offset:0 lds\n\t"
      "buffer_load_dwordx4 v85, %[rq], %[tmp] offen offset:1024 lds\n\t"
      "buffer_load_dwordx4 v86, %[rq], %[tmp] offen offset:2048 lds\n\t"
      "buffer_load_dwordx4 v87, %[rq], %[tmp] offen offset:3072 lds\n\t"
      "s_mov_b64 exec, -1\n\t"
      "s_waitcnt vmcnt(8) lgkmcnt(0)\n\t"
      "s_barrier\n\t"
      "s_add_u32 %[tmp], %[r0], 5\n\t"
      "s_and_b32 %[tmp], %[tmp], 7\n\t"
      "s_mul_i32 %[tmp], %[tmp], 128\n\t"
      "s_add_u32 m0, %[dst], 66560\n\t"
      "s_nop 0\n\t"
      "buffer_load_dwordx4 v80, %[rx], %[tmp] offen offset:0 lds\n\t"
      "buffer_load_dwordx4 v81, %[rx], %[tmp] offen offset:1024 lds\n\t"
      "buffer_load_dwordx4 v82, %[rx], %[tmp] offen offset:2048 lds\n\t"
      "buffer_load_dwordx4 v83, %[rx], %[tmp] offen offset:3072 lds\n\t"
      "ds_read_b128 v[176:179], v92 offset:0\n\t"
      "ds_read_b128 v[180:183], v93 offset:0\n\t"
      "ds_read_b128 v[184:187], v94 offset:0\n\t"
      "ds_read_b128 v[188:191], v95 offset:0\n\t"
      "s_add_u32 %[tmp], %[r0], 5\n\t"
      "s_and_b32 %[tmp], %[tmp], 7\n\t"
      "s_mul_i32 %[tmp], %[tmp], 196608\n\t"
      "s_add_u32 m0, %[dst], 133120\n\t"
      "s_nop 0\n\t"
      "s_mov_b32 exec_lo, 0xf0ff0fff\n\t"
      "s_mov_b32 exec_hi, 0xfff0ff0f\n\t"
      "buffer_load_dwordx4 v84, %[rq], %[tmp] offen offset:0 lds\n\t"
      "buffer_load_dwordx4 v85, %[rq], %[tmp] offen offset:1024 lds\n\t"
      "buffer_load_dwordx4 v86, %[rq], %[tmp] offen offset:2048 lds\n\t"
      "buffer_load_dwordx4 v87, %[rq], %[tmp] offen offset:3072 lds\n\t"
      "s_mov_b64 exec, -1\n\t"
      "s_waitcnt vmcnt(8) lgkmcnt(0)\n\t"
      "s_barrier\n\t"
      "s_add_u32 %[tmp], %[r0], 6\n\t"
      "s_and_b32 %[tmp], %[tmp], 7\n\t"
      "s_mul_i32 %[tmp], %[tmp], 128\n\t"
      "s_add_u32 m0, %[dst], 33280\n\t"
      "s_nop 0\n\t"
      "buffer_load_dwordx4 v80, %[rx], %[tmp] offen offset:0 lds\n\t"
      "buffer_load_dwordx4 v81, %[rx], %[tmp] offen offset:1024 lds\n\t"
      "buffer_load_dwordx4 v82, %[rx], %[tmp] offen offset:2048 lds\n\t"
      "buffer_load_dwordx4 v83, %[rx], %[tmp] offen offset:3072 lds\n\t"
      "ds_read_b128 v[192:195], v92 offset:16640\n\t"
      "ds_read_b128 v[196:199], v93 offset:16640\n\t"
      "ds_read_b128 v[200:203], v94 offset:16640\n\t"
      "ds_read_b128 v[204:207], v95 offset:16640\n\t"
      "s_add_u32 %[tmp], %[r0], 6\n\t"
      "s_and_b32 %[tmp], %[tmp], 7\n\t"
      "s_mul_i32 %[tmp], %[tmp], 196608\n\t"
      "s_add_u32 m0, %[dst], 99840\n\t"
      "s_nop 0\n\t"
      "s_mov_b32 exec_lo, 0xf0ff0fff\n\t"
      "s_mov_b32 exec_hi, 0xfff0ff0f\n\t"
      "buffer_load_dwordx4 v84, %[rq], %[tmp] offen offset:0 lds\n\t"
      "buffer_load_dwordx4 v85, %[rq], %[tmp] offen offset:1024 lds\n\t"
      "buffer_load_dwordx4 v86, %[rq], %[tmp] offen offset:2048 lds\n\t"
      "buffer_load_dwordx4 v87, %[rq], %[tmp] offen offset:3072 lds\n\t"
      "s_mov_b64 exec, -1\n\t"
      "s_waitcnt vmcnt(8) lgkmcnt(0)\n\t"
      "s_barrier\n\t"
      "s_add_u32 %[tmp], %[r0], 7\n\t"
      "s_and_b32 %[tmp], %[tmp], 7\n\t"
      "s_mul_i32 %[tmp], %[tmp], 128\n\t"
      "s_add_u32 m0, %[dst], 49920\n\t"
      "s_nop 0\n\t"
      "buffer_load_dwordx4 v80, %[rx], %[tmp] offen offset:0 lds\n\t"
      "buffer_load_dwordx4 v81, %[rx], %[tmp] offen offset:1024 lds\n\t"
      "buffer_load_dwordx4 v82, %[rx], %[tmp] offen offset:2048 lds\n\t"
      "buffer_load_dwordx4 v83, %[rx], %[tmp] offen offset:3072 lds\n\t"
      "ds_read_b128 v[208:211], v92 offset:33280\n\t"
      "ds_read_b128 v[212:215], v93 offset:33280\n\t"
      "ds_read_b128 v[216:219], v94 offset:33280\n\t"
      "ds_read_b128 v[220:223], v95 offset:33280\n\t"
      "s_add_u32 %[tmp], %[r0], 7\n\t"
      "s_and_b32 %[tmp], %[tmp], 7\n\t"
      "s_mul_i32 %[tmp], %[tmp], 196608\n\t"
      "s_add_u32 m0, %[dst], 116480\n\t"
      "s_nop 0\n\t"
      "s_mov_b32 exec_lo, 0xf0ff0fff\n\t"
      "s_mov_b32 exec_hi, 0xfff0ff0f\n\t"
      "buffer_load_dwordx4 v84, %[rq], %[tmp] offen offset:0 lds\n\t"
      "buffer_load_dwordx4 v85, %[rq], %[tmp] offen offset:1024 lds\n\t"
      "buffer_load_dwordx4 v86, %[rq], %[tmp] offen offset:2048 lds\n\t"
      "buffer_load_dwordx4 v87, %[rq], %[tmp] offen offset:3072 lds\n\t"
      "s_mov_b64 exec, -1\n\t"
      "s_waitcnt vmcnt(8) lgkmcnt(0)\n\t"
      "s_barrier\n\t"
      "ds_read_b128 v[224:227], v92 offset:0\n\t"
      "ds_read_b128 v[228:231], v93 offset:0\n\t"
      "ds_read_b128 v[232:235], v94 offset:0\n\t"
      "ds_read_b128 v[236:239], v95 offset:0\n\t"
      "s_cmp_lt_i32 %[job2], 0\n\t"
      "s_cbranch_scc1 5f\n\t"
      "s_add_u32 %[tmp], %[r0], 2\n\t"
      "s_and_b32 %[tmp], %[tmp], 7\n\t"
      "s_mul_i32 %[tmp], %[tmp], 229376\n\t"
      "s_add_u32 m0, %[dst], 66560\n\t"
      "s_nop 0\n\t"
      "buffer_load_dwordx4 v88, %[rf], %[tmp] offen offset:0 lds\n\t"
      "buffer_load_dwordx4 v89, %[rf], %[tmp] offen offset:1024 lds\n\t"
      "buffer_load_dwordx4 v90, %[rf], %[tmp] offen offset:2048 lds\n\t"
      "buffer_load_dwordx4 v91, %[rf], %[tmp] offen offset:3072 lds\n\t"
      "5:\n\t"
      "v_xor_b32 v80, 0, %[wo]\n\t"
      "v_add_u32 v80, %[bh], v80\n\t"
      "v_xor_b32 v81, 16, %[wo]\n\t"
      "v_add_u32 v81, %[bh], v81\n\t"
      "v_xor_b32 v82, 64, %[wo]\n\t"
      "v_add_u32 v82, %[bh], v82\n\t"
      "v_xor_b32 v83, 80, %[wo]\n\t"
      "v_add_u32 v83, %[bh], v83\n\t"
      "v_xor_b32 v84, 128, %[wo]\n\t"
      "v_add_u32 v84, %[bh], v84\n\t"
      "v_xor_b32 v85, 144, %[wo]\n\t"
      "v_add_u32 v85, %[bh], v85\n\t"
      "v_xor_b32 v86, 192, %[wo]\n\t"
      "v_add_u32 v86, %[bh], v86\n\t"
      "v_xor_b32 v87, 208, %[wo]\n\t"
      "v_add_u32 v87, %[bh], v87\n\t"
      "s_cmp_ge_i32 %[job2], 0\n\t"
      "s_cbranch_scc1 4f\n\t"
      "s_waitcnt vmcnt(0)\n\t"
      "4:\n\t"
      "s_waitcnt vmcnt(4) lgkmcnt(0)\n\t"
      "s_barrier\n\t"
      "ds_read_b128 v[240:243], v92 offset:16640\n\t"
      "ds_read_b128 v[244:247], v93 offset:16640\n\t"
      "ds_read_b128 v[248:251], v94 offset:16640\n\t"
      "ds_read_b128 v[252:255], v95 offset:16640\n\t"
      "s_setprio 1\n\t"
      "s_cmp_lt_i32 %[job2], 0\n\t"
      "s_cbranch_scc1 8f\n\t"
      "ds_read_b64_tr_b16 v[96:97], v80 offset:0\n\t"
      "ds_read_b64_tr_b16 v[98:99], v81 offset:1024\n\t"
      "ds_read_b64_tr_b16 v[100:101], v82 offset:0\n\t"
      "ds_read_b64_tr_b16 v[102:103], v83 offset:1024\n\t"
      "ds_read_b64_tr_b16 v[104:105], v84 offset:0\n\t"
      "ds_read_b64_tr_b16 v[106:107], v85 offset:1024\n\t"
      "ds_read_b64_tr_b16 v[108:109], v86 offset:0\n\t"
      "ds_read_b64_tr_b16 v[110:111], v87 offset:1024\n\t"
      "ds_read_b64_tr_b16 v[112:113], v80 offset:4096\n\t"
      "ds_read_b64_tr_b16 v[114:115], v81 offset:5120\n\t"
      "ds_read_b64_tr_b16 v[116:117], v82 offset:4096\n\t"
      "ds_read_b64_tr_b16 v[118:119], v83 offset:5120\n\t"
      "ds_read_b64_tr_b16 v[120:121], v84 offset:4096\n\t"
      "ds_read_b64_tr_b16 v[122:123], v85 offset:5120\n\t"
      "ds_read_b64_tr_b16 v[124:125], v86 offset:4096\n\t"
      "ds_read_b64_tr_b16 v[126:127], v87 offset:5120\n\t"
      "s_waitcnt lgkmcnt(8)\n\t"
      "v_mfma_f32_32x32x16_bf16 %[c0], v[96:99], v[128:131], 0\n\t"
      "ds_read_b64_tr_b16 v[96:97], v80 offset:8192\n\t"
      "ds_read_b64_tr_b16 v[98:99], v81 offset:9216\n\t"
      "s_waitcnt vmcnt(4)\n\t"
      "s_mov_b64 exec, 1\n\t"
      "ds_add_u32 %[cb], v79 offset:0\n\t"
      "s_mov_b64 exec, -1\n\t"
      "v_mfma_f32_32x32x16_bf16 %[c1], v[100:103], v[128:131], 0\n\t"
      "ds_read_b64_tr_b16 v[100:101], v82 offset:8192\n\t"
      "ds_read_b64_tr_b16 v[102:103], v83 offset:9216\n\t"
      "v_mfma_f32_32x32x16_bf16 %[c2], v[104:107], v[128:131], 0\n\t"
      "ds_read_b64_tr_b16 v[104:105], v84 offset:8192\n\t"
      "ds_read_b64_tr_b16 v[106:107], v85 offset:9216\n\t"
      "s_add_u32 %[tm2], %[r0], 3\n\t"
      "s_and_b32 %[tm2], %[tm2], 7\n\t"
      "s_mul_i32 %[tm2], %[tm2], 229376\n\t"
      "s_add_u32 m0, %[dst], 133120\n\t"
      "s_nop 0\n\t"
      "v_mfma_f32_32x32x16_bf16 %[c3], v[108:111], v[128:131], 0\n\t"
      "ds_read_b64_tr_b16 v[108:109], v86 offset:8192\n\t"
      "ds_read_b64_tr_b16 v[110:111], v87 offset:9216\n\t"
      "buffer_load_dwordx4 v88, %[rf], %[tm2] offen offset:0 lds\n\t"
      "s_waitcnt lgkmcnt(9)\n\t"
      "v_mfma_f32_32x32x16_bf16 %[c0], v[112:115], v[132:135], %[c0]\n\t"
      "ds_read_b64_tr_b16 v[112:113], v80 offset:12288\n\t"
      "ds_read_b64_tr_b16 v[114:115], v81 offset:13312\n\t"
      "v_mfma_f32_32x32x16_bf16 %[c1], v[116:119], v[132:135], %[c1]\n\t"
      "ds_read_b64_tr_b16 v[116:117], v82 offset:12288\n\t"
      "ds_read_b64_tr_b16 v[118:119], v83 offset:13312\n\t"
      "v_mfma_f32_32x32x16_bf16 %[c2], v[120:123], v[132:135], %[c2]\n\t"
      "ds_read_b64_tr_b16 v[120:121], v84 offset:12288\n\t"
      "ds_read_b64_tr_b16 v[122:123], v85 offset:13312\n\t"
      "v_mfma_f32_32x32x16_bf16 %[c3], v[124:127], v[132:135], %[c3]\n\t"
      "ds_read_b64_tr_b16 v[124:125], v86 offset:12288\n\t"
      "ds_read_b64_tr_b16 v[126:127], v87 offset:13312\n\t"
      "buffer_load_dwordx4 v89, %[rf], %[tm2] offen offset:1024 lds\n\t"
      "s_waitcnt lgkmcnt(8)\n\t"
      "v_mfma_f32_32x32x16_bf16 %[c0], v[96:99], v[136:139], %[c0]\n\t"
      "v_add_u32 v80, -66560, v80\n\t"
      "v_add_u32 v81, -66560, v81\n\t"
      "ds_read_b64_tr_b16 v[96:97], v80 offset:0\n\t"
      "ds_read_b64_tr_b16 v[98:99], v81 offset:1024\n\t"
      "v_mfma_f32_32x32x16_bf16 %[c1], v[100:103], v[136:139], %[c1]\n\t"
      "v_add_u32 v82, -66560, v82\n\t"
      "v_add_u32 v83, -66560, v83\n\t"
      "ds_read_b64_tr_b16 v[100:101], v82 offset:0\n\t"
      "ds_read_b64_tr_b16 v[102:103], v83 offset:1024\n\t"
      "v_mfma_f32_32x32x16_bf16 %[c2], v[104:107], v[136:139], %[c2]\n\t"
      "v_add_u32 v84, -66560, v84\n\t"
      "v_add_u32 v85, -66560, v85\n\t"
      "ds_read_b64_tr_b16 v[104:105], v84 offset:0\n\t"
      "ds_read_b64_tr_b16 v[106:107], v85 offset:1024\n\t"
      "v_mfma_f32_32x32x16_bf16 %[c3], v[108:111], v[136:139], %[c3]\n\t"
      "v_add_u32 v86, -66560, v86\n\t"
      "v_add_u32 v87, -66560, v87\n\t"
      "ds_read_b64_tr_b16 v[108:109], v86 offset:0\n\t"
      "ds_read_b64_tr_b16 v[110:111], v87 offset:1024\n\t"
      "buffer_load_dwordx4 v90, %[rf], %[tm2] offen offset:2048 lds\n\t"
      "s_waitcnt lgkmcnt(8)\n\t"
      "v_mfma_f32_32x32x16_bf16 %[c0], v[112:115], v[140:143], %[c0]\n\t"
      "ds_read_b64_tr_b16 v[112:113], v80 offset:4096\n\t"
      "ds_read_b64_tr_b16 v[114:115], v81 offset:5120\n\t"
      "v_mfma_f32_32x32x16_bf16 %[c1], v[116:119], v[140:143], %[c1]\n\t"
      "ds_read_b64_tr_b16 v[116:117], v82 offset:4096\n\t"
      "ds_read_b64_tr_b16 v[118:119], v83 offset:5120\n\t"
      "v_mfma_f32_32x32x16_bf16 %[c2], v[120:123], v[140:143], %[c2]\n\t"
      "ds_read_b64_tr_b16 v[120:121], v84 offset:4096\n\t"
      "ds_read_b64_tr_b16 v[122:123], v85 offset:5120\n\t"
      "v_mfma_f32_32x32x16_bf16 %[c3], v[124:127], v[140:143], %[c3]\n\t"
      "ds_read_b64_tr_b16 v[124:125], v86 offset:4096\n\t"
      "ds_read_b64_tr_b16 v[126:127], v87 offset:5120\n\t"
      "buffer_load_dwordx4 v91, %[rf], %[tm2] offen offset:3072 lds\n\t"
      "s_waitcnt lgkmcnt(8)\n\t"
      "v_mfma_f32_32x32x16_bf16 %[c0], v[96:99], v[144:147], %[c0]\n\t"
      "ds_read_b64_tr_b16 v[96:97], v80 offset:8192\n\t"
      "ds_read_b64_tr_b16 v[98:99], v81 offset:9216\n\t"
      "s_waitcnt vmcnt(4)\n\t"
      "s_mov_b64 exec, 1\n\t"
      "ds_add_u32 %[cb], v79 offset:0\n\t"
      "s_mov_b64 exec, -1\n\t"
      "v_mfma_f32_32x32x16_bf16 %[c1], v[100:103], v[144:147], %[c1]\n\t"
      "ds_read_b64_tr_b16 v[100:101], v82 offset:8192\n\t"
      "ds_read_b64_tr_b16 v[102:103], v83 offset:9216\n\t"
      "v_mfma_f32_32x32x16_bf16 %[c2], v[104:107], v[144:147], %[c2]\n\t"
      "ds_read_b64_tr_b16 v[104:105], v84 offset:8192\n\t"
      "ds_read_b64_tr_b16 v[106:107], v85 offset:9216\n\t"
      "s_add_u32 %[tm2], %[r0], 4\n\t"
      "s_and_b32 %[tm2], %[tm2], 7\n\t"
      "s_mul_i32 %[tm2], %[tm2], 229376\n\t"
      "s_add_u32 m0, %[dst], 99840\n\t"
      "s_nop 0\n\t"
      "v_mfma_f32_32x32x16_bf16 %[c3], v[108:111], v[144:147], %[c3]\n\t"
      "ds_read_b64_tr_b16 v[108:109], v86 offset:8192\n\t"
      "ds_read_b64_tr_b16 v[110:111], v87 offset:9216\n\t"
      "buffer_load_dwordx4 v88, %[rf], %[tm2] offen offset:0 lds\n\t"
      "s_waitcnt lgkmcnt(9)\n\t"
      "v_mfma_f32_32x32x16_bf16 %[c0], v[112:115], v[148:151], %[c0]\n\t"
      "ds_read_b64_tr_b16 v[112:113], v80 offset:12288\n\t"
      "ds_read_b64_tr_b16 v[114:115], v81 offset:13312\n\t"
      "v_mfma_f32_32x32x16_bf16 %[c1], v[116:119], v[148:151], %[c1]\n\t"
      "ds_read_b64_tr_b16 v[116:117], v82 offset:12288\n\t"
      "ds_read_b64_tr_b16 v[118:119], v83 offset:13312\n\t"
      "v_mfma_f32_32x32x16_bf16 %[c2], v[120:123], v[148:151], %[c2]\n\t"
      "ds_read_b64_tr_b16 v[120:121], v84 offset:12288\n\t"
      "ds_read_b64_tr_b16 v[122:123], v85 offset:13312\n\t"
      "ds_read_b32 v78, %[cb] offset:0\n\t"
      "v_mfma_f32_32x32x16_bf16 %[c3], v[124:127], v[148:151], %[c3]\n\t"
      "ds_read_b64_tr_b16 v[124:125], v86 offset:12288\n\t"
      "ds_read_b64_tr_b16 v[126:127], v87 offset:13312\n\t"
      "buffer_load_dwordx4 v89, %[rf], %[tm2] offen offset:1024 lds\n\t"
      "s_waitcnt lgkmcnt(9)\n\t"
      "v_mfma_f32_32x32x16_bf16 %[c0], v[96:99], v[152:155], %[c0]\n\t"
      "v_mfma_f32_32x32x16_bf16 %[c1], v[100:103], v[152:155], %[c1]\n\t"
      "s_waitcnt lgkmcnt(2)\n\t"
      "v_readfirstlane_b32 %[tmp], v78\n\t"
      "s_nop 0\n\t"
      "s_cmp_ge_u32 %[tmp], 8\n\t"
      "s_cbranch_scc1 11f\n\t"
      "10:\n\t"
      "ds_read_b32 v78, %[cb] offset:0\n\t"
      "s_waitcnt lgkmcnt(0)\n\t"
      "v_readfirstlane_b32 %[tmp], v78\n\t"
      "s_nop 3\n\t"
      "s_cmp_lt_u32 %[tmp], 8\n\t"
      "s_cbranch_scc1 10b\n\t"
      "11:\n\t"
      "v_mfma_f32_32x32x16_bf16 %[c2], v[104:107], v[152:155], %[c2]\n\t"
      "ds_read_b64_tr_b16 v[96:97], v80 offset:49920\n\t"
      "ds_read_b64_tr_b16 v[98:99], v81 offset:50944\n\t"
      "ds_read_b64_tr_b16 v[100:101], v82 offset:49920\n\t"
      "ds_read_b64_tr_b16 v[102:103], v83 offset:50944\n\t"
      "ds_read_b64_tr_b16 v[104:105], v84 offset:49920\n\t"
      "ds_read_b64_tr_b16 v[106:107], v85 offset:50944\n\t"
      "v_mfma_f32_32x32x16_bf16 %[c3], v[108:111], v[152:155], %[c3]\n\t"
      "ds_read_b64_tr_b16 v[108:109], v86 offset:49920\n\t"
      "ds_read_b64_tr_b16 v[110:111], v87 offset:50944\n\t"
      "buffer_load_dwordx4 v90, %[rf], %[tm2] offen offset:2048 lds\n\t"
      "s_waitcnt lgkmcnt(8)\n\t"
      "v_mfma_f32_32x32x16_bf16 %[c0], v[112:115], v[156:159], %[c0]\n\t"
      "ds_read_b64_tr_b16 v[112:113], v80 offset:54016\n\t"
      "ds_read_b64_tr_b16 v[114:115], v81 offset:55040\n\t"
      "v_mfma_f32_32x32x16_bf16 %[c1], v[116:119], v[156:159], %[c1]\n\t"
      "ds_read_b64_tr_b16 v[116:117], v82 offset:54016\n\t"
      "ds_read_b64_tr_b16 v[118:119], v83 offset:55040\n\t"
      "v_mfma_f32_32x32x16_bf16 %[c2], v[120:123], v[156:159], %[c2]\n\t"
      "ds_read_b64_tr_b16 v[120:121], v84 offset:54016\n\t"
      "ds_read_b64_tr_b16 v[122:123], v85 offset:55040\n\t"
      "v_mfma_f32_32x32x16_bf16 %[c3], v[124:127], v[156:159], %[c3]\n\t"
      "ds_read_b64_tr_b16 v[124:125], v86 offset:54016\n\t"
      "ds_read_b64_tr_b16 v[126:127], v87 offset:55040\n\t"
      "buffer_load_dwordx4 v91, %[rf], %[tm2] offen offset:3072 lds\n\t"
      "s_waitcnt lgkmcnt(8)\n\t"
      "v_mfma_f32_32x32x16_bf16 %[c0], v[96:99], v[160:163], %[c0]\n\t"
      "ds_read_b64_tr_b16 v[96:97], v80 offset:58112\n\t"
      "ds_read_b64_tr_b16 v[98:99], v81 offset:59136\n\t"
      "s_waitcnt vmcnt(4)\n\t"
      "s_mov_b64 exec, 1\n\t"
      "ds_add_u32 %[cb], v79 offset:0\n\t"
      "s_mov_b64 exec, -1\n\t"
      "v_mfma_f32_32x32x16_bf16 %[c1], v[100:103], v[160:163], %[c1]\n\t"
      "ds_read_b64_tr_b16 v[100:101], v82 offset:58112\n\t"
      "ds_read_b64_tr_b16 v[102:103], v83 offset:59136\n\t"
      "v_mfma_f32_32x32x16_bf16 %[c2], v[104:107], v[160:163], %[c2]\n\t"
      "ds_read_b64_tr_b16 v[104:105], v84 offset:58112\n\t"
      "ds_read_b64_tr_b16 v[106:107], v85 offset:59136\n\t"
      "s_add_u32 %[tm2], %[r0], 5\n\t"
      "s_and_b32 %[tm2], %[tm2], 7\n\t"
      "s_mul_i32 %[tm2], %[tm2], 229376\n\t"
      "s_add_u32 m0, %[dst], 33280\n\t"
      "s_nop 0\n\t"
      "v_mfma_f32_32x32x16_bf16 %[c3], v[108:111], v[160:163], %[c3]\n\t"
      "ds_read_b64_tr_b16 v[108:109], v86 offset:58112\n\t"
      "ds_read_b64_tr_b16 v[110:111], v87 offset:59136\n\t"
      "buffer_load_dwordx4 v88, %[rf], %[tm2] offen offset:0 lds\n\t"
      "s_waitcnt lgkmcnt(9)\n\t"
      "v_mfma_f32_32x32x16_bf16 %[c0], v[112:115], v[164:167], %[c0]\n\t"
      "ds_read_b64_tr_b16 v[112:113], v80 offset:62208\n\t"
      "ds_read_b64_tr_b16 v[114:115], v81 offset:63232\n\t"
      "v_mfma_f32_32x32x16_bf16 %[c1], v[116:119], v[164:167], %[c1]\n\t"
      "ds_read_b64_tr_b16 v[116:117], v82 offset:62208\n\t"
      "ds_read_b64_tr_b16 v[118:119], v83 offset:63232\n\t"
      "v_mfma_f32_32x32x16_bf16 %[c2], v[120:123], v[164:167], %[c2]\n\t"
      "ds_read_b64_tr_b16 v[120:121], v84 offset:62208\n\t"
      "ds_read_b64_tr_b16 v[122:123], v85 offset:63232\n\t"
      "ds_read_b32 v78, %[cb] offset:0\n\t"
      "v_mfma_f32_32x32x16_bf16 %[c3], v[124:127], v[164:167], %[c3]\n\t"
      "ds_read_b64_tr_b16 v[124:125], v86 offset:62208\n\t"
      "ds_read_b64_tr_b16 v[126:127], v87 offset:63232\n\t"
      "buffer_load_dwordx4 v89, %[rf], %[tm2] offen offset:1024 lds\n\t"
      "s_waitcnt lgkmcnt(9)\n\t"
      "v_mfma_f32_32x32x16_bf16 %[c0], v[96:99], v[168:171], %[c0]\n\t"
      "v_mfma_f32_32x32x16_bf16 %[c1], v[100:103], v[168:171], %[c1]\n\t"
      "s_waitcnt lgkmcnt(2)\n\t"
      "v_readfirstlane_b32 %[tmp], v78\n\t"
      "s_nop 0\n\t"
      "s_cmp_ge_u32 %[tmp], 12\n\t"
      "s_cbranch_scc1 13f\n\t"
      "12:\n\t"
      "ds_read_b32 v78, %[cb] offset:0\n\t"
      "s_waitcnt lgkmcnt(0)\n\t"
      "v_readfirstlane_b32 %[tmp], v78\n\t"
      "s_nop 3\n\t"
      "s_cmp_lt_u32 %[tmp], 12\n\t"
      "s_cbranch_scc1 12b\n\t"
      "13:\n\t"
      "v_mfma_f32_32x32x16_bf16 %[c2], v[104:107], v[168:171], %[c2]\n\t"
      "v_add_u32 v80, 66560, v80\n\t"
      "v_add_u32 v81, 66560, v81\n\t"
      "ds_read_b64_tr_b16 v[96:97], v80 offset:49920\n\t"
      "ds_read_b64_tr_b16 v[98:99], v81 offset:50944\n\t"
      "v_add_u32 v82, 66560, v82\n\t"
      "v_add_u32 v83, 66560, v83\n\t"
      "ds_read_b64_tr_b16 v[100:101], v82 offset:49920\n\t"
      "ds_read_b64_tr_b16 v[102:103], v83 offset:50944\n\t"
      "v_add_u32 v84, 66560, v84\n\t"
      "v_add_u32 v85, 66560, v85\n\t"
      "ds_read_b64_tr_b16 v[104:105], v84 offset:49920\n\t"
      "ds_read_b64_tr_b16 v[106:107], v85 offset:50944\n\t"
      "v_mfma_f32_32x32x16_bf16 %[c3], v[108:111], v[168:171], %[c3]\n\t"
      "v_add_u32 v86, 66560, v86\n\t"
      "v_add_u32 v87, 66560, v87\n\t"
      "ds_read_b64_tr_b16 v[108:109], v86 offset:49920\n\t"
      "ds_read_b64_tr_b16 v[110:111], v87 offset:50944\n\t"
      "buffer_load_dwordx4 v90, %[rf], %[tm2] offen offset:2048 lds\n\t"
      "s_waitcnt lgkmcnt(8)\n\t"
      "v_mfma_f32_32x32x16_bf16 %[c0], v[112:115], v[172:175], %[c0]\n\t"
      "ds_read_b64_tr_b16 v[112:113], v80 offset:54016\n\t"
      "ds_read_b64_tr_b16 v[114:115], v81 offset:55040\n\t"
      "v_mfma_f32_32x32x16_bf16 %[c1], v[116:119], v[172:175], %[c1]\n\t"
      "ds_read_b64_tr_b16 v[116:117], v82 offset:54016\n\t"
      "ds_read_b64_tr_b16 v[118:119], v83 offset:55040\n\t"
      "v_mfma_f32_32x32x16_bf16 %[c2], v[120:123], v[172:175], %[c2]\n\t"
      "ds_read_b64_tr_b16 v[120:121], v84 offset:54016\n\t"
      "ds_read_b64_tr_b16 v[122:123], v85 offset:55040\n\t"
      "v_mfma_f32_32x32x16_bf16 %[c3], v[124:127], v[172:175], %[c3]\n\t"
      "ds_read_b64_tr_b16 v[124:125], v86 offset:54016\n\t"
      "ds_read_b64_tr_b16 v[126:127], v87 offset:55040\n\t"
      "buffer_load_dwordx4 v91, %[rf], %[tm2] offen offset:3072 lds\n\t"
      "s_waitcnt lgkmcnt(8)\n\t"
      "v_mfma_f32_32x32x16_bf16 %[c0], v[96:99], v[176:179], %[c0]\n\t"
      "ds_read_b64_tr_b16 v[96:97], v80 offset:58112\n\t"
      "ds_read_b64_tr_b16 v[98:99], v81 offset:59136\n\t"
      "s_waitcnt vmcnt(4)\n\t"
      "s_mov_b64 exec, 1\n\t"
      "ds_add_u32 %[cb], v79 offset:0\n\t"
      "s_mov_b64 exec, -1\n\t"
      "v_mfma_f32_32x32x16_bf16 %[c1], v[100:103], v[176:179], %[c1]\n\t"
      "ds_read_b64_tr_b16 v[100:101], v82 offset:58112\n\t"
      "ds_read_b64_tr_b16 v[102:103], v83 offset:59136\n\t"
      "14:\n\t"
      "ds_read_b32 v78, %[cb] offset:4\n\t"
      "s_waitcnt lgkmcnt(0)\n\t"
      "v_readfirstlane_b32 %[tmp], v78\n\t"
      "s_nop 3\n\t"
      "s_cmp_lt_u32 %[tmp], 4\n\t"
      "s_cbranch_scc1 14b\n\t"
      "v_mfma_f32_32x32x16_bf16 %[c2], v[104:107], v[176:179], %[c2]\n\t"
      "ds_read_b64_tr_b16 v[104:105], v84 offset:58112\n\t"
      "ds_read_b64_tr_b16 v[106:107], v85 offset:59136\n\t"
      "s_add_u32 %[tm2], %[r0], 6\n\t"
      "s_and_b32 %[tm2], %[tm2], 7\n\t"
      "s_mul_i32 %[tm2], %[tm2], 229376\n\t"
      "s_add_u32 m0, %[dst], 49920\n\t"
      "s_nop 0\n\t"
      "v_mfma_f32_32x32x16_bf16 %[c3], v[108:111], v[176:179], %[c3]\n\t"
      "ds_read_b64_tr_b16 v[108:109], v86 offset:58112\n\t"
      "ds_read_b64_tr_b16 v[110:111], v87 offset:59136\n\t"
      "buffer_load_dwordx4 v88, %[rf], %[tm2] offen offset:0 lds\n\t"
      "v_mfma_f32_32x32x16_bf16 %[c0], v[112:115], v[180:183], %[c0]\n\t"
      "ds_read_b64_tr_b16 v[112:113], v80 offset:62208\n\t"
      "ds_read_b64_tr_b16 v[114:115], v81 offset:63232\n\t"
      "v_mfma_f32_32x32x16_bf16 %[c1], v[116:119], v[180:183], %[c1]\n\t"
      "ds_read_b64_tr_b16 v[116:117], v82 offset:62208\n\t"
      "ds_read_b64_tr_b16 v[118:119], v83 offset:63232\n\t"
      "v_mfma_f32_32x32x16_bf16 %[c2], v[120:123], v[180:183], %[c2]\n\t"
      "ds_read_b64_tr_b16 v[120:121], v84 offset:62208\n\t"
      "ds_read_b64_tr_b16 v[122:123], v85 offset:63232\n\t"
      "ds_read_b32 v78, %[cb] offset:0\n\t"
      "v_mfma_f32_32x32x16_bf16 %[c3], v[124:127], v[180:183], %[c3]\n\t"
      "ds_read_b64_tr_b16 v[124:125], v86 offset:62208\n\t"
      "ds_read_b64_tr_b16 v[126:127], v87 offset:63232\n\t"
      "buffer_load_dwordx4 v89, %[rf], %[tm2] offen offset:1024 lds\n\t"
      "s_waitcnt lgkmcnt(9)\n\t"
      "v_mfma_f32_32x32x16_bf16 %[c0], v[96:99], v[184:187], %[c0]\n\t"
      "v_mfma_f32_32x32x16_bf16 %[c1], v[100:103], v[184:187], %[c1]\n\t"
      "s_waitcnt lgkmcnt(2)\n\t"
      "v_readfirstlane_b32 %[tmp], v78\n\t"
      "s_nop 0\n\t"
      "s_cmp_ge_u32 %[tmp], 16\n\t"
      "s_cbranch_scc1 16f\n\t"
      "15:\n\t"
      "ds_read_b32 v78, %[cb] offset:0\n\t"
      "s_waitcnt lgkmcnt(0)\n\t"
      "v_readfirstlane_b32 %[tmp], v78\n\t"
      "s_nop 3\n\t"
      "s_cmp_lt_u32 %[tmp], 16\n\t"
      "s_cbranch_scc1 15b\n\t"
      "16:\n\t"
      "v_mfma_f32_32x32x16_bf16 %[c2], v[104:107], v[184:187], %[c2]\n\t"
      "ds_read_b64_tr_b16 v[96:97], v80 offset:16640\n\t"
      "ds_read_b64_tr_b16 v[98:99], v81 offset:17664\n\t"
      "ds_read_b64_tr_b16 v[100:101], v82 offset:16640\n\t"
      "ds_read_b64_tr_b16 v[102:103], v83 offset:17664\n\t"
      "ds_read_b64_tr_b16 v[104:105], v84 offset:16640\n\t"
      "ds_read_b64_tr_b16 v[106:107], v85 offset:17664\n\t"
      "v_mfma_f32_32x32x16_bf16 %[c3], v[108:111], v[184:187], %[c3]\n\t"
      "ds_read_b64_tr_b16 v[108:109], v86 offset:16640\n\t"
      "ds_read_b64_tr_b16 v[110:111], v87 offset:17664\n\t"
      "buffer_load_dwordx4 v90, %[rf], %[tm2] offen offset:2048 lds\n\t"
      "s_waitcnt lgkmcnt(8)\n\t"
      "v_mfma_f32_32x32x16_bf16 %[c0], v[112:115], v[188:191], %[c0]\n\t"
      "ds_read_b64_tr_b16 v[112:113], v80 offset:20736\n\t"
      "ds_read_b64_tr_b16 v[114:115], v81 offset:21760\n\t"
      "v_mfma_f32_32x32x16_bf16 %[c1], v[116:119], v[188:191], %[c1]\n\t"
      "ds_read_b64_tr_b16 v[116:117], v82 offset:20736\n\t"
      "ds_read_b64_tr_b16 v[118:119], v83 offset:21760\n\t"
      "v_mfma_f32_32x32x16_bf16 %[c2], v[120:123], v[188:191], %[c2]\n\t"
      "ds_read_b64_tr_b16 v[120:121], v84 offset:20736\n\t"
      "ds_read_b64_tr_b16 v[122:123], v85 offset:21760\n\t"
      "v_mfma_f32_32x32x16_bf16 %[c3], v[124:127], v[188:191], %[c3]\n\t"
      "ds_read_b64_tr_b16 v[124:125], v86 offset:20736\n\t"
      "ds_read_b64_tr_b16 v[126:127], v87 offset:21760\n\t"
      "buffer_load_dwordx4 v91, %[rf], %[tm2] offen offset:3072 lds\n\t"
      "s_waitcnt lgkmcnt(8)\n\t"
      "v_mfma_f32_32x32x16_bf16 %[c0], v[96:99], v[192:195], %[c0]\n\t"
      "ds_read_b64_tr_b16 v[96:97], v80 offset:24832\n\t"
      "ds_read_b64_tr_b16 v[98:99], v81 offset:25856\n\t"
      "s_waitcnt vmcnt(4)\n\t"
      "s_mov_b64 exec, 1\n\t"
      "ds_add_u32 %[cb], v79 offset:0\n\t"
      "s_mov_b64 exec, -1\n\t"
      "v_mfma_f32_32x32x16_bf16 %[c1], v[100:103], v[192:195], %[c1]\n\t"
      "ds_read_b64_tr_b16 v[100:101], v82 offset:24832\n\t"
      "ds_read_b64_tr_b16 v[102:103], v83 offset:25856\n\t"
      "v_mfma_f32_32x32x16_bf16 %[c2], v[104:107], v[192:195], %[c2]\n\t"
      "ds_read_b64_tr_b16 v[104:105], v84 offset:24832\n\t"
      "ds_read_b64_tr_b16 v[106:107], v85 offset:25856\n\t"
      "s_add_u32 %[tm2], %[r0], 7\n\t"
      "s_and_b32 %[tm2], %[tm2], 7\n\t"
      "s_mul_i32 %[tm2], %[tm2], 229376\n\t"
      "s_add_u32 m0, %[dst], 116480\n\t"
      "s_nop 0\n\t"
      "v_mfma_f32_32x32x16_bf16 %[c3], v[108:111], v[192:195], %[c3]\n\t"
      "ds_read_b64_tr_b16 v[108:109], v86 offset:24832\n\t"
      "ds_read_b64_tr_b16 v[110:111], v87 offset:25856\n\t"
      "buffer_load_dwordx4 v88, %[rf], %[tm2] offen offset:0 lds\n\t"
      "s_waitcnt lgkmcnt(9)\n\t"
      "v_mfma_f32_32x32x16_bf16 %[c0], v[112:115], v[196:199], %[c0]\n\t"
      "ds_read_b64_tr_b16 v[112:113], v80 offset:28928\n\t"
      "ds_read_b64_tr_b16 v[114:115], v81 offset:29952\n\t"
      "v_mfma_f32_32x32x16_bf16 %[c1], v[116:119], v[196:199], %[c1]\n\t"
      "ds_read_b64_tr_b16 v[116:117], v82 offset:28928\n\t"
      "ds_read_b64_tr_b16 v[118:119], v83 offset:29952\n\t"
      "v_mfma_f32_32x32x16_bf16 %[c2], v[120:123], v[196:199], %[c2]\n\t"
      "ds_read_b64_tr_b16 v[120:121], v84 offset:28928\n\t"
      "ds_read_b64_tr_b16 v[122:123], v85 offset:29952\n\t"
      "ds_read_b32 v78, %[cb] offset:0\n\t"
      "v_mfma_f32_32x32x16_bf16 %[c3], v[124:127], v[196:199], %[c3]\n\t"
      "ds_read_b64_tr_b16 v[124:125], v86 offset:28928\n\t"
      "ds_read_b64_tr_b16 v[126:127], v87 offset:29952\n\t"
      "buffer_load_dwordx4 v89, %[rf], %[tm2] offen offset:1024 lds\n\t"
      "s_waitcnt lgkmcnt(9)\n\t"
      "v_mfma_f32_32x32x16_bf16 %[c0], v[96:99], v[200:203], %[c0]\n\t"
      "v_mfma_f32_32x32x16_bf16 %[c1], v[100:103], v[200:203], %[c1]\n\t"
      "s_waitcnt lgkmcnt(2)\n\t"
      "v_readfirstlane_b32 %[tmp], v78\n\t"
      "s_nop 0\n\t"
      "s_cmp_ge_u32 %[tmp], 20\n\t"
      "s_cbranch_scc1 18f\n\t"
      "17:\n\t"
      "ds_read_b32 v78, %[cb] offset:0\n\t"
      "s_waitcnt lgkmcnt(0)\n\t"
      "v_readfirstlane_b32 %[tmp], v78\n\t"
      "s_nop 3\n\t"
      "s_cmp_lt_u32 %[tmp], 20\n\t"
      "s_cbranch_scc1 17b\n\t"
      "18:\n\t"
      "v_mfma_f32_32x32x16_bf16 %[c2], v[104:107], v[200:203], %[c2]\n\t"
      "v_add_u32 v80, -66560, v80\n\t"
      "v_add_u32 v81, -66560, v81\n\t"
      "ds_read_b64_tr_b16 v[96:97], v80 offset:16640\n\t"
      "ds_read_b64_tr_b16 v[98:99], v81 offset:17664\n\t"
      "v_add_u32 v82, -66560, v82\n\t"
      "v_add_u32 v83, -66560, v83\n\t"
      "ds_read_b64_tr_b16 v[100:101], v82 offset:16640\n\t"
      "ds_read_b64_tr_b16 v[102:103], v83 offset:17664\n\t"
      "v_add_u32 v84, -66560, v84\n\t"
      "v_add_u32 v85, -66560, v85\n\t"
      "ds_read_b64_tr_b16 v[104:105], v84 offset:16640\n\t"
      "ds_read_b64_tr_b16 v[106:107], v85 offset:17664\n\t"
      "v_mfma_f32_32x32x16_bf16 %[c3], v[108:111], v[200:203], %[c3]\n\t"
      "v_add_u32 v86, -66560, v86\n\t"
      "v_add_u32 v87, -66560, v87\n\t"
      "ds_read_b64_tr_b16 v[108:109], v86 offset:16640\n\t"
      "ds_read_b64_tr_b16 v[110:111], v87 offset:17664\n\t"
      "buffer_load_dwordx4 v90, %[rf], %[tm2] offen offset:2048 lds\n\t"
      "s_waitcnt lgkmcnt(8)\n\t"
      "v_mfma_f32_32x32x16_bf16 %[c0], v[112:115], v[204:207], %[c0]\n\t"
      "ds_read_b64_tr_b16 v[112:113], v80 offset:20736\n\t"
      "ds_read_b64_tr_b16 v[114:115], v81 offset:21760\n\t"
      "v_mfma_f32_32x32x16_bf16 %[c1], v[116:119], v[204:207], %[c1]\n\t"
      "ds_read_b64_tr_b16 v[116:117], v82 offset:20736\n\t"
      "ds_read_b64_tr_b16 v[118:119], v83 offset:21760\n\t"
      "v_mfma_f32_32x32x16_bf16 %[c2], v[120:123], v[204:207], %[c2]\n\t"
      "ds_read_b64_tr_b16 v[120:121], v84 offset:20736\n\t"
      "ds_read_b64_tr_b16 v[122:123], v85 offset:21760\n\t"
      "v_mfma_f32_32x32x16_bf16 %[c3], v[124:127], v[204:207], %[c3]\n\t"
      "ds_read_b64_tr_b16 v[124:125], v86 offset:20736\n\t"
      "ds_read_b64_tr_b16 v[126:127], v87 offset:21760\n\t"
      "buffer_load_dwordx4 v91, %[rf], %[tm2] offen offset:3072 lds\n\t"
      "s_waitcnt lgkmcnt(8)\n\t"
      "v_mfma_f32_32x32x16_bf16 %[c0], v[96:99], v[208:211], %[c0]\n\t"
      "ds_read_b64_tr_b16 v[96:97], v80 offset:24832\n\t"
      "ds_read_b64_tr_b16 v[98:99], v81 offset:25856\n\t"
      "s_waitcnt vmcnt(4)\n\t"
      "s_mov_b64 exec, 1\n\t"
      "ds_add_u32 %[cb], v79 offset:0\n\t"
      "s_mov_b64 exec, -1\n\t"
      "v_mfma_f32_32x32x16_bf16 %[c1], v[100:103], v[208:211], %[c1]\n\t"
      "ds_read_b64_tr_b16 v[100:101], v82 offset:24832\n\t"
      "ds_read_b64_tr_b16 v[102:103], v83 offset:25856\n\t"
      "v_mfma_f32_32x32x16_bf16 %[c2], v[104:107], v[208:211], %[c2]\n\t"
      "ds_read_b64_tr_b16 v[104:105], v84 offset:24832\n\t"
      "ds_read_b64_tr_b16 v[106:107], v85 offset:25856\n\t"
      "v_mfma_f32_32x32x16_bf16 %[c3], v[108:111], v[208:211], %[c3]\n\t"
      "ds_read_b64_tr_b16 v[108:109], v86 offset:24832\n\t"
      "ds_read_b64_tr_b16 v[110:111], v87 offset:25856\n\t"
      "s_waitcnt lgkmcnt(9)\n\t"
      "v_mfma_f32_32x32x16_bf16 %[c0], v[112:115], v[212:215], %[c0]\n\t"
      "ds_read_b64_tr_b16 v[112:113], v80 offset:28928\n\t"
      "ds_read_b64_tr_b16 v[114:115], v81 offset:29952\n\t"
      "v_mfma_f32_32x32x16_bf16 %[c1], v[116:119], v[212:215], %[c1]\n\t"
      "ds_read_b64_tr_b16 v[116:117], v82 offset:28928\n\t"
      "ds_read_b64_tr_b16 v[118:119], v83 offset:29952\n\t"
      "v_mfma_f32_32x32x16_bf16 %[c2], v[120:123], v[212:215], %[c2]\n\t"
      "ds_read_b64_tr_b16 v[120:121], v84 offset:28928\n\t"
      "ds_read_b64_tr_b16 v[122:123], v85 offset:29952\n\t"
      "ds_read_b32 v78, %[cb] offset:0\n\t"
      "v_mfma_f32_32x32x16_bf16 %[c3], v[124:127], v[212:215], %[c3]\n\t"
      "ds_read_b64_tr_b16 v[124:125], v86 offset:28928\n\t"
      "ds_read_b64_tr_b16 v[126:127], v87 offset:29952\n\t"
      "s_waitcnt lgkmcnt(9)\n\t"
      "v_mfma_f32_32x32x16_bf16 %[c0], v[96:99], v[216:219], %[c0]\n\t"
      "v_mfma_f32_32x32x16_bf16 %[c1], v[100:103], v[216:219], %[c1]\n\t"
      "s_waitcnt lgkmcnt(2)\n\t"
      "v_readfirstlane_b32 %[tmp], v78\n\t"
      "s_nop 0\n\t"
      "s_cmp_ge_u32 %[tmp], 24\n\t"
      "s_cbranch_scc1 20f\n\t"
      "19:\n\t"
      "ds_read_b32 v78, %[cb] offset:0\n\t"
      "s_waitcnt lgkmcnt(0)\n\t"
      "v_readfirstlane_b32 %[tmp], v78\n\t"
      "s_nop 3\n\t"
      "s_cmp_lt_u32 %[tmp], 24\n\t"
      "s_cbranch_scc1 19b\n\t"
      "20:\n\t"
      "v_mfma_f32_32x32x16_bf16 %[c2], v[104:107], v[216:219], %[c2]\n\t"
      "ds_read_b64_tr_b16 v[96:97], v80 offset:33280\n\t"
      "ds_read_b64_tr_b16 v[98:99], v81 offset:34304\n\t"
      "ds_read_b64_tr_b16 v[100:101], v82 offset:33280\n\t"
      "ds_read_b64_tr_b16 v[102:103], v83 offset:34304\n\t"
      "ds_read_b64_tr_b16 v[104:105], v84 offset:33280\n\t"
      "ds_read_b64_tr_b16 v[106:107], v85 offset:34304\n\t"
      "v_mfma_f32_32x32x16_bf16 %[c3], v[108:111], v[216:219], %[c3]\n\t"
      "ds_read_b64_tr_b16 v[108:109], v86 offset:33280\n\t"
      "ds_read_b64_tr_b16 v[110:111], v87 offset:34304\n\t"
      "s_waitcnt lgkmcnt(8)\n\t"
      "v_mfma_f32_32x32x16_bf16 %[c0], v[112:115], v[220:223], %[c0]\n\t"
      "ds_read_b64_tr_b16 v[112:113], v80 offset:37376\n\t"
      "ds_read_b64_tr_b16 v[114:115], v81 offset:38400\n\t"
      "v_mfma_f32_32x32x16_bf16 %[c1], v[116:119], v[220:223], %[c1]\n\t"
      "ds_read_b64_tr_b16 v[116:117], v82 offset:37376\n\t"
      "ds_read_b64_tr_b16 v[118:119], v83 offset:38400\n\t"
      "v_mfma_f32_32x32x16_bf16 %[c2], v[120:123], v[220:223], %[c2]\n\t"
      "ds_read_b64_tr_b16 v[120:121], v84 offset:37376\n\t"
      "ds_read_b64_tr_b16 v[122:123], v85 offset:38400\n\t"
      "v_mfma_f32_32x32x16_bf16 %[c3], v[124:127], v[220:223], %[c3]\n\t"
      "ds_read_b64_tr_b16 v[124:125], v86 offset:37376\n\t"
      "ds_read_b64_tr_b16 v[126:127], v87 offset:38400\n\t"
      "s_waitcnt lgkmcnt(8)\n\t"
      "v_mfma_f32_32x32x16_bf16 %[c0], v[96:99], v[224:227], %[c0]\n\t"
      "ds_read_b64_tr_b16 v[96:97], v80 offset:41472\n\t"
      "ds_read_b64_tr_b16 v[98:99], v81 offset:42496\n\t"
      "s_waitcnt vmcnt(0)\n\t"
      "s_mov_b64 exec, 1\n\t"
      "ds_add_u32 %[cb], v79 offset:0\n\t"
      "s_mov_b64 exec, -1\n\t"
      "v_mfma_f32_32x32x16_bf16 %[c1], v[100:103], v[224:227], %[c1]\n\t"
      "ds_read_b64_tr_b16 v[100:101], v82 offset:41472\n\t"
      "ds_read_b64_tr_b16 v[102:103], v83 offset:42496\n\t"
      "v_mfma_f32_32x32x16_bf16 %[c2], v[104:107], v[224:227], %[c2]\n\t"
      "ds_read_b64_tr_b16 v[104:105], v84 offset:41472\n\t"
      "ds_read_b64_tr_b16 v[106:107], v85 offset:42496\n\t"
      "v_mfma_f32_32x32x16_bf16 %[c3], v[108:111], v[224:227], %[c3]\n\t"
      "ds_read_b64_tr_b16 v[108:109], v86 offset:41472\n\t"
      "ds_read_b64_tr_b16 v[110:111], v87 offset:42496\n\t"
      "s_waitcnt lgkmcnt(9)\n\t"
      "v_mfma_f32_32x32x16_bf16 %[c0], v[112:115], v[228:231], %[c0]\n\t"
      "ds_read_b64_tr_b16 v[112:113], v80 offset:45568\n\t"
      "ds_read_b64_tr_b16 v[114:115], v81 offset:46592\n\t"
      "v_mfma_f32_32x32x16_bf16 %[c1], v[116:119], v[228:231], %[c1]\n\t"
      "ds_read_b64_tr_b16 v[116:117], v82 offset:45568\n\t"
      "ds_read_b64_tr_b16 v[118:119], v83 offset:46592\n\t"
      "v_mfma_f32_32x32x16_bf16 %[c2], v[120:123], v[228:231], %[c2]\n\t"
      "ds_read_b64_tr_b16 v[120:121], v84 offset:45568\n\t"
      "ds_read_b64_tr_b16 v[122:123], v85 offset:46592\n\t"
      "ds_read_b32 v78, %[cb] offset:0\n\t"
      "v_mfma_f32_32x32x16_bf16 %[c3], v[124:127], v[228:231], %[c3]\n\t"
      "ds_read_b64_tr_b16 v[124:125], v86 offset:45568\n\t"
      "ds_read_b64_tr_b16 v[126:127], v87 offset:46592\n\t"
      "s_waitcnt lgkmcnt(9)\n\t"
      "v_mfma_f32_32x32x16_bf16 %[c0], v[96:99], v[232:235], %[c0]\n\t"
      "v_mfma_f32_32x32x16_bf16 %[c1], v[100:103], v[232:235], %[c1]\n\t"
      "s_waitcnt lgkmcnt(2)\n\t"
      "v_readfirstlane_b32 %[tmp], v78\n\t"
      "s_nop 0\n\t"
      "s_cmp_ge_u32 %[tmp], 28\n\t"
      "s_cbranch_scc1 22f\n\t"
      "21:\n\t"
      "ds_read_b32 v78, %[cb] offset:0\n\t"
      "s_waitcnt lgkmcnt(0)\n\t"
      "v_readfirstlane_b32 %[tmp], v78\n\t"
      "s_nop 3\n\t"
      "s_cmp_lt_u32 %[tmp], 28\n\t"
      "s_cbranch_scc1 21b\n\t"
      "22:\n\t"
      "v_mfma_f32_32x32x16_bf16 %[c2], v[104:107], v[232:235], %[c2]\n\t"
      "v_add_u32 v80, 66560, v80\n\t"
      "v_add_u32 v81, 66560, v81\n\t"
      "ds_read_b64_tr_b16 v[96:97], v80 offset:33280\n\t"
      "ds_read_b64_tr_b16 v[98:99], v81 offset:34304\n\t"
      "v_add_u32 v82, 66560, v82\n\t"
      "v_add_u32 v83, 66560, v83\n\t"
      "ds_read_b64_tr_b16 v[100:101], v82 offset:33280\n\t"
      "ds_read_b64_tr_b16 v[102:103], v83 offset:34304\n\t"
      "v_add_u32 v84, 66560, v84\n\t"
      "v_add_u32 v85, 66560, v85\n\t"
      "ds_read_b64_tr_b16 v[104:105], v84 offset:33280\n\t"
      "ds_read_b64_tr_b16 v[106:107], v85 offset:34304\n\t"
      "v_mfma_f32_32x32x16_bf16 %[c3], v[108:111], v[232:235], %[c3]\n\t"
      "v_add_u32 v86, 66560, v86\n\t"
      "v_add_u32 v87, 66560, v87\n\t"
      "ds_read_b64_tr_b16 v[108:109], v86 offset:33280\n\t"
      "ds_read_b64_tr_b16 v[110:111], v87 offset:34304\n\t"
      "s_waitcnt lgkmcnt(8)\n\t"
      "v_mfma_f32_32x32x16_bf16 %[c0], v[112:115], v[236:239], %[c0]\n\t"
      "ds_read_b64_tr_b16 v[112:113], v80 offset:37376\n\t"
      "ds_read_b64_tr_b16 v[114:115], v81 offset:38400\n\t"
      "v_mfma_f32_32x32x16_bf16 %[c1], v[116:119], v[236:239], %[c1]\n\t"
      "ds_read_b64_tr_b16 v[116:117], v82 offset:37376\n\t"
      "ds_read_b64_tr_b16 v[118:119], v83 offset:38400\n\t"
      "v_mfma_f32_32x32x16_bf16 %[c2], v[120:123], v[236:239], %[c2]\n\t"
      "ds_read_b64_tr_b16 v[120:121], v84 offset:37376\n\t"
      "ds_read_b64_tr_b16 v[122:123], v85 offset:38400\n\t"
      "v_mfma_f32_32x32x16_bf16 %[c3], v[124:127], v[236:239], %[c3]\n\t"
      "ds_read_b64_tr_b16 v[124:125], v86 offset:37376\n\t"
      "ds_read_b64_tr_b16 v[126:127], v87 offset:38400\n\t"
      "s_waitcnt lgkmcnt(8)\n\t"
      "v_mfma_f32_32x32x16_bf16 %[c0], v[96:99], v[240:243], %[c0]\n\t"
      "ds_read_b64_tr_b16 v[96:97], v80 offset:41472\n\t"
      "ds_read_b64_tr_b16 v[98:99], v81 offset:42496\n\t"
      "v_mfma_f32_32x32x16_bf16 %[c1], v[100:103], v[240:243], %[c1]\n\t"
      "ds_read_b64_tr_b16 v[100:101], v82 offset:41472\n\t"
      "ds_read_b64_tr_b16 v[102:103], v83 offset:42496\n\t"
      "v_mfma_f32_32x32x16_bf16 %[c2], v[104:107], v[240:243], %[c2]\n\t"
      "ds_read_b64_tr_b16 v[104:105], v84 offset:41472\n\t"
      "ds_read_b64_tr_b16 v[106:107], v85 offset:42496\n\t"
      "v_mfma_f32_32x32x16_bf16 %[c3], v[108:111], v[240:243], %[c3]\n\t"
      "ds_read_b64_tr_b16 v[108:109], v86 offset:41472\n\t"
      "ds_read_b64_tr_b16 v[110:111], v87 offset:42496\n\t"
      "s_waitcnt lgkmcnt(8)\n\t"
      "v_mfma_f32_32x32x16_bf16 %[c0], v[112:115], v[244:247], %[c0]\n\t"
      "ds_read_b64_tr_b16 v[112:113], v80 offset:45568\n\t"
      "ds_read_b64_tr_b16 v[114:115], v81 offset:46592\n\t"
      "v_mfma_f32_32x32x16_bf16 %[c1], v[116:119], v[244:247], %[c1]\n\t"
      "ds_read_b64_tr_b16 v[116:117], v82 offset:45568\n\t"
      "ds_read_b64_tr_b16 v[118:119], v83 offset:46592\n\t"
      "v_mfma_f32_32x32x16_bf16 %[c2], v[120:123], v[244:247], %[c2]\n\t"
      "ds_read_b64_tr_b16 v[120:121], v84 offset:45568\n\t"
      "ds_read_b64_tr_b16 v[122:123], v85 offset:46592\n\t"
      "v_mfma_f32_32x32x16_bf16 %[c3], v[124:127], v[244:247], %[c3]\n\t"
      "ds_read_b64_tr_b16 v[124:125], v86 offset:45568\n\t"
      "ds_read_b64_tr_b16 v[126:127], v87 offset:46592\n\t"
      "s_waitcnt lgkmcnt(8)\n\t"
      "v_mfma_f32_32x32x16_bf16 %[c0], v[96:99], v[248:251], %[c0]\n\t"
      "v_mfma_f32_32x32x16_bf16 %[c1], v[100:103], v[248:251], %[c1]\n\t"
      "v_mfma_f32_32x32x16_bf16 %[c2], v[104:107], v[248:251], %[c2]\n\t"
      "v_mfma_f32_32x32x16_bf16 %[c3], v[108:111], v[248:251], %[c3]\n\t"
      "s_waitcnt lgkmcnt(0)\n\t"
      "v_mfma_f32_32x32x16_bf16 %[c0], v[112:115], v[252:255], %[c0]\n\t"
      "v_mfma_f32_32x32x16_bf16 %[c1], v[116:119], v[252:255], %[c1]\n\t"
      "v_mfma_f32_32x32x16_bf16 %[c2], v[120:123], v[252:255], %[c2]\n\t"
      "v_mfma_f32_32x32x16_bf16 %[c3], v[124:127], v[252:255], %[c3]\n\t"
      "s_branch 9f\n\t"
      "8:\n\t"
      "v_mov_b32 v79, 8\n\t"
      "s_cmp_lg_i32 %[job2], -2\n\t"
      "s_cbranch_scc1 9f\n\t"
      "v_mov_b32 v80, 0\n\t"
      "v_mov_b32 v81, 0\n\t"
      "v_mov_b32 v82, 0\n\t"
      "v_mov_b32 v83, 0\n\t"
      "s_add_u32 %[tmp], %[r0], 0\n\t"
      "s_and_b32 %[tmp], %[tmp], 7\n\t"
      "s_lshl_b32 %[tmp], %[tmp], 7\n\t"
      "v_add_u32 v78, %[tmp], %[ga]\n\t"
      "ds_read_b128 v[96:99], v78 offset:0\n\t"
      "ds_read_b128 v[100:103], v78 offset:32\n\t"
      "ds_read_b128 v[104:107], v78 offset:64\n\t"
      "ds_read_b128 v[108:111], v78 offset:96\n\t"
      "s_add_u32 %[tmp], %[r0], 1\n\t"
      "s_and_b32 %[tmp], %[tmp], 7\n\t"
      "s_lshl_b32 %[tmp], %[tmp], 7\n\t"
      "v_add_u32 v78, %[tmp], %[ga]\n\t"
      "ds_read_b128 v[112:115], v78 offset:0\n\t"
      "ds_read_b128 v[116:119], v78 offset:32\n\t"
      "ds_read_b128 v[120:123], v78 offset:64\n\t"
      "ds_read_b128 v[124:127], v78 offset:96\n\t"
      "s_waitcnt lgkmcnt(4)\n\t"
      "v_dot2c_f32_bf16 v80, v96, v128\n\t"
      "v_dot2c_f32_bf16 v81, v97, v129\n\t"
      "v_dot2c_f32_bf16 v82, v98, v130\n\t"
      "v_dot2c_f32_bf16 v83, v99, v131\n\t"
      "v_dot2c_f32_bf16 v80, v100, v132\n\t"
      "v_dot2c_f32_bf16 v81, v101, v133\n\t"
      "v_dot2c_f32_bf16 v82, v102, v134\n\t"
      "v_dot2c_f32_bf16 v83, v103, v135\n\t"
      "v_dot2c_f32_bf16 v80, v104, v136\n\t"
      "v_dot2c_f32_bf16 v81, v105, v137\n\t"
      "v_dot2c_f32_bf16 v82, v106, v138\n\t"
      "v_dot2c_f32_bf16 v83, v107, v139\n\t"
      "v_dot2c_f32_bf16 v80, v108, v140\n\t"
      "v_dot2c_f32_bf16 v81, v109, v141\n\t"
      "v_dot2c_f32_bf16 v82, v110, v142\n\t"
      "v_dot2c_f32_bf16 v83, v111, v143\n\t"
      "s_add_u32 %[tmp], %[r0], 2\n\t"
      "s_and_b32 %[tmp], %[tmp], 7\n\t"
      "s_lshl_b32 %[tmp], %[tmp], 7\n\t"
      "v_add_u32 v78, %[tmp], %[ga]\n\t"
      "ds_read_b128 v[96:99], v78 offset:0\n\t"
      "ds_read_b128 v[100:103], v78 offset:32\n\t"
      "ds_read_b128 v[104:107], v78 offset:64\n\t"
      "ds_read_b128 v[108:111], v78 offset:96\n\t"
      "s_waitcnt lgkmcnt(4)\n\t"
      "v_dot2c_f32_bf16 v80, v112, v144\n\t"
      "v_dot2c_f32_bf16 v81, v113, v145\n\t"
      "v_dot2c_f32_bf16 v82, v114, v146\n\t"
      "v_dot2c_f32_bf16 v83, v115, v147\n\t"
      "v_dot2c_f32_bf16 v80, v116, v148\n\t"
      "v_dot2c_f32_bf16 v81, v117, v149\n\t"
      "v_dot2c_f32_bf16 v82, v118, v150\n\t"
      "v_dot2c_f32_bf16 v83, v119, v151\n\t"
      "v_dot2c_f32_bf16 v80, v120, v152\n\t"
      "v_dot2c_f32_bf16 v81, v121, v153\n\t"
      "v_dot2c_f32_bf16 v82, v122, v154\n\t"
      "v_dot2c_f32_bf16 v83, v123, v155\n\t"
      "v_dot2c_f32_bf16 v80, v124, v156\n\t"
      "v_dot2c_f32_bf16 v81, v125, v157\n\t"
      "v_dot2c_f32_bf16 v82, v126, v158\n\t"
      "v_dot2c_f32_bf16 v83, v127, v159\n\t"
      "s_add_u32 %[tmp], %[r0], 3\n\t"
      "s_and_b32 %[tmp], %[tmp], 7\n\t"
      "s_lshl_b32 %[tmp], %[tmp], 7\n\t"
      "v_add_u32 v78, %[tmp], %[ga]\n\t"
      "ds_read_b128 v[112:115], v78 offset:0\n\t"
      "ds_read_b128 v[116:119], v78 offset:32\n\t"
      "ds_read_b128 v[120:123], v78 offset:64\n\t"
      "ds_read_b128 v[124:127], v78 offset:96\n\t"
      "s_waitcnt lgkmcnt(4)\n\t"
      "v_dot2c_f32_bf16 v80, v96, v160\n\t"
      "v_dot2c_f32_bf16 v81, v97, v161\n\t"
      "v_dot2c_f32_bf16 v82, v98, v162\n\t"
      "v_dot2c_f32_bf16 v83, v99, v163\n\t"
      "v_dot2c_f32_bf16 v80, v100, v164\n\t"
      "v_dot2c_f32_bf16 v81, v101, v165\n\t"
      "v_dot2c_f32_bf16 v82, v102, v166\n\t"
      "v_dot2c_f32_bf16 v83, v103, v167\n\t"
      "v_dot2c_f32_bf16 v80, v104, v168\n\t"
      "v_dot2c_f32_bf16 v81, v105, v169\n\t"
      "v_dot2c_f32_bf16 v82, v106, v170\n\t"
      "v_dot2c_f32_bf16 v83, v107, v171\n\t"
      "v_dot2c_f32_bf16 v80, v108, v172\n\t"
      "v_dot2c_f32_bf16 v81, v109, v173\n\t"
      "v_dot2c_f32_bf16 v82, v110, v174\n\t"
      "v_dot2c_f32_bf16 v83, v111, v175\n\t"
      "s_add_u32 %[tmp], %[r0], 4\n\t"
      "s_and_b32 %[tmp], %[tmp], 7\n\t"
      "s_lshl_b32 %[tmp], %[tmp], 7\n\t"
      "v_add_u32 v78, %[tmp], %[ga]\n\t"
      "ds_read_b128 v[96:99], v78 offset:0\n\t"
      "ds_read_b128 v[100:103], v78 offset:32\n\t"
      "ds_read_b128 v[104:107], v78 offset:64\n\t"
      "ds_read_b128 v[108:111], v78 offset:96\n\t"
      "s_waitcnt lgkmcnt(4)\n\t"
      "v_dot2c_f32_bf16 v80, v112, v176\n\t"
      "v_dot2c_f32_bf16 v81, v113, v177\n\t"
      "v_dot2c_f32_bf16 v82, v114, v178\n\t"
      "v_dot2c_f32_bf16 v83, v115, v179\n\t"
      "v_dot2c_f32_bf16 v80, v116, v180\n\t"
      "v_dot2c_f32_bf16 v81, v117, v181\n\t"
      "v_dot2c_f32_bf16 v82, v118, v182\n\t"
      "v_dot2c_f32_bf16 v83, v119, v183\n\t"
      "v_dot2c_f32_bf16 v80, v120, v184\n\t"
      "v_dot2c_f32_bf16 v81, v121, v185\n\t"
      "v_dot2c_f32_bf16 v82, v122, v186\n\t"
      "v_dot2c_f32_bf16 v83, v123, v187\n\t"
      "v_dot2c_f32_bf16 v80, v124, v188\n\t"
      "v_dot2c_f32_bf16 v81, v125, v189\n\t"
      "v_dot2c_f32_bf16 v82, v126, v190\n\t"
      "v_dot2c_f32_bf16 v83, v127, v191\n\t"
      "s_add_u32 %[tmp], %[r0], 5\n\t"
      "s_and_b32 %[tmp], %[tmp], 7\n\t"
      "s_lshl_b32 %[tmp], %[tmp], 7\n\t"
      "v_add_u32 v78, %[tmp], %[ga]\n\t"
      "ds_read_b128 v[112:115], v78 offset:0\n\t"
      "ds_read_b128 v[116:119], v78 offset:32\n\t"
      "ds_read_b128 v[120:123], v78 offset:64\n\t"
      "ds_read_b128 v[124:127], v78 offset:96\n\t"
      "s_waitcnt lgkmcnt(4)\n\t"
      "v_dot2c_f32_bf16 v80, v96, v192\n\t"
      "v_dot2c_f32_bf16 v81, v97, v193\n\t"
      "v_dot2c_f32_bf16 v82, v98, v194\n\t"
      "v_dot2c_f32_bf16 v83, v99, v195\n\t"
      "v_dot2c_f32_bf16 v80, v100, v196\n\t"
      "v_dot2c_f32_bf16 v81, v101, v197\n\t"
      "v_dot2c_f32_bf16 v82, v102, v198\n\t"
      "v_dot2c_f32_bf16 v83, v103, v199\n\t"
      "v_dot2c_f32_bf16 v80, v104, v200\n\t"
      "v_dot2c_f32_bf16 v81, v105, v201\n\t"
      "v_dot2c_f32_bf16 v82, v106, v202\n\t"
      "v_dot2c_f32_bf16 v83, v107, v203\n\t"
      "v_dot2c_f32_bf16 v80, v108, v204\n\t"
      "v_dot2c_f32_bf16 v81, v109, v205\n\t"
      "v_dot2c_f32_bf16 v82, v110, v206\n\t"
      "v_dot2c_f32_bf16 v83, v111, v207\n\t"
      "s_add_u32 %[tmp], %[r0], 6\n\t"
      "s_and_b32 %[tmp], %[tmp], 7\n\t"
      "s_lshl_b32 %[tmp], %[tmp], 7\n\t"
      "v_add_u32 v78, %[tmp], %[ga]\n\t"
      "ds_read_b128 v[96:99], v78 offset:0\n\t"
      "ds_read_b128 v[100:103], v78 offset:32\n\t"
      "ds_read_b128 v[104:107], v78 offset:64\n\t"
      "ds_read_b128 v[108:111], v78 offset:96\n\t"
      "s_waitcnt lgkmcnt(4)\n\t"
      "v_dot2c_f32_bf16 v80, v112, v208\n\t"
      "v_dot2c_f32_bf16 v81, v113, v209\n\t"
      "v_dot2c_f32_bf16 v82, v114, v210\n\t"
      "v_dot2c_f32_bf16 v83, v115, v211\n\t"
      "v_dot2c_f32_bf16 v80, v116, v212\n\t"
      "v_dot2c_f32_bf16 v81, v117, v213\n\t"
      "v_dot2c_f32_bf16 v82, v118, v214\n\t"
      "v_dot2c_f32_bf16 v83, v119, v215\n\t"
      "v_dot2c_f32_bf16 v80, v120, v216\n\t"
      "v_dot2c_f32_bf16 v81, v121, v217\n\t"
      "v_dot2c_f32_bf16 v82, v122, v218\n\t"
      "v_dot2c_f32_bf16 v83, v123, v219\n\t"
      "v_dot2c_f32_bf16 v80, v124, v220\n\t"
      "v_dot2c_f32_bf16 v81, v125, v221\n\t"
      "v_dot2c_f32_bf16 v82, v126, v222\n\t"
      "v_dot2c_f32_bf16 v83, v127, v223\n\t"
      "s_add_u32 %[tmp], %[r0], 7\n\t"
      "s_and_b32 %[tmp], %[tmp], 7\n\t"
      "s_lshl_b32 %[tmp], %[tmp], 7\n\t"
      "v_add_u32 v78, %[tmp], %[ga]\n\t"
      "ds_read_b128 v[112:115], v78 offset:0\n\t"
      "ds_read_b128 v[116:119], v78 offset:32\n\t"
      "ds_read_b128 v[120:123], v78 offset:64\n\t"
      "ds_read_b128 v[124:127], v78 offset:96\n\t"
      "s_waitcnt lgkmcnt(4)\n\t"
      "v_dot2c_f32_bf16 v80, v96, v224\n\t"
      "v_dot2c_f32_bf16 v81, v97, v225\n\t"
      "v_dot2c_f32_bf16 v82, v98, v226\n\t"
      "v_dot2c_f32_bf16 v83, v99, v227\n\t"
      "v_dot2c_f32_bf16 v80, v100, v228\n\t"
      "v_dot2c_f32_bf16 v81, v101, v229\n\t"
      "v_dot2c_f32_bf16 v82, v102, v230\n\t"
      "v_dot2c_f32_bf16 v83, v103, v231\n\t"
      "v_dot2c_f32_bf16 v80, v104, v232\n\t"
      "v_dot2c_f32_bf16 v81, v105, v233\n\t"
      "v_dot2c_f32_bf16 v82, v106, v234\n\t"
      "v_dot2c_f32_bf16 v83, v107, v235\n\t"
      "v_dot2c_f32_bf16 v80, v108, v236\n\t"
      "v_dot2c_f32_bf16 v81, v109, v237\n\t"
      "v_dot2c_f32_bf16 v82, v110, v238\n\t"
      "v_dot2c_f32_bf16 v83, v111, v239\n\t"
      "s_waitcnt lgkmcnt(0)\n\t"
      "v_dot2c_f32_bf16 v80, v112, v240\n\t"
      "v_dot2c_f32_bf16 v81, v113, v241\n\t"
      "v_dot2c_f32_bf16 v82, v114, v242\n\t"
      "v_dot2c_f32_bf16 v83, v115, v243\n\t"
      "v_dot2c_f32_bf16 v80, v116, v244\n\t"
      "v_dot2c_f32_bf16 v81, v117, v245\n\t"
      "v_dot2c_f32_bf16 v82, v118, v246\n\t"
      "v_dot2c_f32_bf16 v83, v119, v247\n\t"
      "v_dot2c_f32_bf16 v80, v120, v248\n\t"
      "v_dot2c_f32_bf16 v81, v121, v249\n\t"
      "v_dot2c_f32_bf16 v82, v122, v250\n\t"
      "v_dot2c_f32_bf16 v83, v123, v251\n\t"
      "v_dot2c_f32_bf16 v80, v124, v252\n\t"
      "v_dot2c_f32_bf16 v81, v125, v253\n\t"
      "v_dot2c_f32_bf16 v82, v126, v254\n\t"
      "v_dot2c_f32_bf16 v83, v127, v255\n\t"
      "s_nop 3\n\t"
      "v_add_f32 v80, v80, v81\n\t"
      "v_add_f32 v82, v82, v83\n\t"
      "v_add_f32 %[gl], v80, v82\n\t"
      "9:\n\t"
      "s_waitcnt lgkmcnt(0)\n\t"
      "s_mov_b64 exec, 1\n\t"
      "ds_add_u32 %[cb], v79 offset:0\n\t"
      "s_mov_b64 exec, -1\n\t"
      "s_nop 8\n\t"
      : [c0] "=&v"(a[0]), [c1] "=&v"(a[1]), [c2] "=&v"(a[2]), [c3] "=&v"(a[3]), [gl] "=&v"(gl), [tmp] "=&s"(tmp), [tm2] "=&s"(tm2), [tag] "=&s"(tag)
      : [xv] "v"((lane >> 3) * 1024 + (((lane & 7) ^ (lane >> 4)) << 4)), [qv] "v"((lane >> 4) * 3072 + c16), [fv] "v"((lane >> 4) * (DFF * 2) + c16),
        [xo] "v"(XO0), [wo] "v"(WO0), [ga] "v"(lds0 + L_GW + h * 16), [cb] "v"(lds0 + L_CTR),
        [rx] "s"(__builtin_amdgcn_make_buffer_rsrc((void*)(x + (size_t)tt * 32 * 1024), 0, 32 * 1024, 0x00020000)),
        [rq] "s"(__builtin_amdgcn_make_buffer_rsrc((void*)(wqkv + woffs), 0, 512 * 3072 - woffs, 0x00020000)),
        [rf] "s"(__builtin_amdgcn_make_buffer_rsrc((void*)(w1 + foffs), 0, 512 * DFF * 2 - foffs, 0x00020000)),
        [dst] "s"(lds0 + rt * 4096), [bl] "s"(lds0 + 2 * SLOT), [bh] "s"(lds0 + 5 * SLOT), [r0] "s"(((bi >> 2) + 2 * rb) & 7), [job2] "s"(job2), [ep] "s"(ws + F_E)
      : "v78", "v79", "v80", "v81", "v82", "v83", "v84", "v85", "v86", "v87", "v88", "v89", "v90", "v91", "v92", "v93", "v94", "v95", "v96", "v97", "v98", "v99", "v100", "v101", "v102", "v103", "v104", "v105", "v106", "v107", "v108", "v109", "v110", "v111", "v112", "v113", "v114", "v115", "v116", "v117", "v118", "v119", "v120", "v121", "v122", "v123", "v124", "v125", "v126", "v127", "v128", "v129", "v130", "v131", "v132", "v133", "v134", "v135", "v136", "v137", "v138", "v139", "v140", "v141", "v142", "v143", "v144", "v145", "v146", "v147", "v148", "v149", "v150", "v151", "v152", "v153", "v154", "v155", "v156", "v157", "v158", "v159", "v160", "v161", "v162", "v163", "v164", "v165", "v166", "v167", "v168", "v169", "v170", "v171", "v172", "v173", "v174", "v175", "v176", "v177", "v178", "v179", "v180", "v181", "v182", "v183", "v184", "v185", "v186", "v187", "v188", "v189", "v190", "v191", "v192", "v193", "v194", "v195", "v196", "v197", "v198", "v199", "v200", "v201", "v202", "v203", "v204", "v205", "v206", "v207", "v208", "v209", "v210", "v211", "v212", "v213", "v214", "v215", "v216", "v217", "v218", "v219", "v220", "v221", "v222", "v223", "v224", "v225", "v226", "v227", "v228", "v229", "v230", "v231", "v232", "v233", "v234", "v235", "v236", "v237", "v238", "v239", "v240", "v241", "v242", "v243", "v244", "v245", "v246", "v247", "v248", "v249", "v250", "v251", "v252", "v253", "v254", "v255", "scc", "memory"); }
    tag += 1;
    const int lane = fresh_lane(), r = lane & 31, h = lane >> 5;
    const unsigned cb = lds0 + L_CTR, l16 = lane * 16;
    const u64 tagv = tag;
    u32x4 hp[8];
    if (job2 >= 0) {
#pragma unroll
      for (int u = 0; u < 8; ++u) hp[u] = max8(pack8(a[u >> 1], u & 1, 1.f), 0);
#pragma unroll
      for (int u = 0; u < 8; ++u) asm volatile("" : "+v"(hp[u]));
    } else {
      if (job2 == -2) {
        const float g = 1.f / (1.f + __expf(-xhalf_sum(gl)));
        if (h == 0) __builtin_amdgcn_raw_buffer_store_b32(__float_as_uint(g), wsr, WS_G + (tt * 32 + r) * 4, 0, 16);
        drain();
        flag_set(wsr, F_2 + (cg * 64 + tt) * FS, tagv, lane * 8);
      }
    }
    lds_wait<C_S, 4>(cb);
    auto kv_block = [&](int j) {
      dma_sc1(l16, wsr, ksrc + j * 8192, lds0 + j * SLOT + rt * 2048); dma_sc1(l16, wsr, ksrc + j * 8192 + 1024, lds0 + j * SLOT + rt * 2048 + 1024);
      dma_sc1(l16, wsr, ksrc + (2u << 20) + j * 8192, lds0 + j * SLOT + 8192 + 2 * rt * VT); dma_sc1(l16, wsr, ksrc + (2u << 20) + j * 8192 + 1024, lds0 + j * SLOT + 8192 + (2 * rt + 1) * VT);
    };
    wait_flags(wsr, lane < 32 ? F_1 + (((lane & 16) ? cgv : cgk) * 64 + KPOS((lane & 14) * 4 + 2 * rt + (lane & 1))) * FS : F_2 + (14 * 64 + qb * 8 + (lane & 7)) * FS, tag);
    lds_wait<C_F, 32>(cb); lds_wait<C_X, 4>(cb);
    kv_block(2); kv_block(3);
    if (lane < 24) ONES_WRITE(2, rt * 24 + lane);
    if (job2 >= 0) {
      const unsigned dst = WS_H + (tt * 112 + job2 * 8) * 1024 + l16;
#pragma unroll
      for (int u = 0; u < 8; ++u) st16(wsr, dst + u * 1024, hp[u]);
      asm volatile("s_waitcnt vmcnt(8) lgkmcnt(0)" ::: "memory");
    } else asm volatile("s_waitcnt vmcnt(0) lgkmcnt(0)" ::: "memory");
    lds_inc<C_K + 4>(cb, 1u);
    kv_block(4); kv_block(5); kv_block(6); kv_block(7);
    if (f == 1) dma_sc1(l16, wsr, WS_G + qb * 1024, lds0 + L_G);
    asm volatile("s_waitcnt vmcnt(16)" ::: "memory");
    if (job2 >= 0) flag_set(wsr, F_2 + (cg * 64 + tt) * FS, tagv, lane * 8);
    drain();
    lds_inc<C_K + 8>(cb, 1u);
    if (b == 0 && f == 1) {
#pragma unroll 1
      for (int k = 0; k < 4; ++k) wait_flags(wsr, F_1 + (k * 64 + lane) * 4 * FS, tag);
      flag_set(wsr, F_E, tagv, lane * 8);
    }
  }
  {
    const int lane = fresh_lane(), r = lane & 31, h = lane >> 5;
    const unsigned rot = (((nb + 2 * (tb & 3)) & 7) * 7) >> 1;
    unsigned sw = rot * 16384, sa = rot * 1024, cnt = 4, cap = SPIN_CAP, tmp;
    const unsigned va0 = WS_H + ((2 * tb) * 112 + 28 * f) * 1024 + lane * 16;
    const int w2o = 28 * f * 16384 + nb * 128;
    asm volatile(
      "v_mov_b32 v43, 1\n\t"
      "buffer_load_dwordx4 v[160:163], %[vw0], %[rw], %[sw] offen\n\t"
      "buffer_load_dwordx4 v[164:167], %[vw1], %[rw], %[sw] offen\n\t"
      "s_add_u32 %[sw], %[sw], 0x4000\n\t"
      "s_cmp_eq_u32 %[sw], 0x70000\n\t"
      "s_cselect_b32 %[sw], 0, %[sw]\n\t"
      "buffer_load_dwordx4 v[168:171], %[vw0], %[rw], %[sw] offen\n\t"
      "buffer_load_dwordx4 v[172:175], %[vw1], %[rw], %[sw] offen\n\t"
      "s_add_u32 %[sw], %[sw], 0x4000\n\t"
      "s_cmp_eq_u32 %[sw], 0x70000\n\t"
      "s_cselect_b32 %[sw], 0, %[sw]\n\t"
      "buffer_load_dwordx4 v[176:179], %[vw0], %[rw], %[sw] offen\n\t"
      "buffer_load_dwordx4 v[180:183], %[vw1], %[rw], %[sw] offen\n\t"
      "s_add_u32 %[sw], %[sw], 0x4000\n\t"
      "s_cmp_eq_u32 %[sw], 0x70000\n\t"
      "s_cselect_b32 %[sw], 0, %[sw]\n\t"
      "buffer_load_dwordx4 v[184:187], %[vw0], %[rw], %[sw] offen\n\t"
      "buffer_load_dwordx4 v[188:191], %[vw1], %[rw], %[sw] offen\n\t"
      "s_add_u32 %[sw], %[sw], 0x4000\n\t"
      "s_cmp_eq_u32 %[sw], 0x70000\n\t"
      "s_cselect_b32 %[sw], 0, %[sw]\n\t"
      "s_mov_b64 exec, 1073741823\n\t"
      "2:\n\t"
      "buffer_load_dwordx2 v[46:47], %[fo], %[ra], 0 offen sc1\n\t"
      "s_waitcnt vmcnt(0)\n\t"
      "v_cmp_ne_u64 vcc, %[tag], v[46:47]\n\t"
      "s_cbranch_vccz 3f\n\t"
      "s_sleep 8\n\t"
      "s_sub_u32 %[cap], %[cap], 1\n\t"
      "s_cmp_lg_u32 %[cap], 0\n\t"
      "s_cbranch_scc1 2b\n\t"
      "3:\n\t"
      "s_mov_b64 exec, -1\n\t"
      "s_setprio 0\n\t"
      "buffer_load_dword v44, %[vg], %[ra], 0 offen sc1\n\t"
      "buffer_load_dword v45, %[vg], %[ra], 0 offen offset:128 sc1\n\t"
      "buffer_load_dwordx4 v[128:131], %[va0], %[ra], %[sa] offen sc1\n\t"
      "buffer_load_dwordx4 v[132:135], %[va1], %[ra], %[sa] offen sc1\n\t"
      "s_add_u32 %[sa], %[sa], 0x400\n\t"
      "s_cmp_eq_u32 %[sa], 0x7000\n\t"
      "s_cselect_b32 %[sa], 0, %[sa]\n\t"
      "buffer_load_dwordx4 v[136:139], %[va0], %[ra], %[sa] offen sc1\n\t"
      "buffer_load_dwordx4 v[140:143], %[va1], %[ra], %[sa] offen sc1\n\t"
      "s_add_u32 %[sa], %[sa], 0x400\n\t"
      "s_cmp_eq_u32 %[sa], 0x7000\n\t"
      "s_cselect_b32 %[sa], 0, %[sa]\n\t"
      "buffer_load_dwordx4 v[144:147], %[va0], %[ra], %[sa] offen sc1\n\t"
      "buffer_load_dwordx4 v[148:151], %[va1], %[ra], %[sa] offen sc1\n\t"
      "s_add_u32 %[sa], %[sa], 0x400\n\t"
      "s_cmp_eq_u32 %[sa], 0x7000\n\t"
      "s_cselect_b32 %[sa], 0, %[sa]\n\t"
      "s_waitcnt vmcnt(12)\n\t"
      "ds_write_b128 %[ww], v[160:163] offset:0\n\t"
      "ds_write_b128 %[ww], v[164:167] offset:1024\n\t"
      "ds_read_b64_tr_b16 v[48:49], %[t0] offset:0\n\t"
      "ds_read_b64_tr_b16 v[50:51], %[t0] offset:1024\n\t"
      "ds_read_b64_tr_b16 v[52:53], %[t1] offset:0\n\t"
      "ds_read_b64_tr_b16 v[54:55], %[t1] offset:1024\n\t"
      "buffer_load_dwordx4 v[160:163], %[vw0], %[rw], %[sw] offen\n\t"
      "buffer_load_dwordx4 v[164:167], %[vw1], %[rw], %[sw] offen\n\t"
      "s_add_u32 %[sw], %[sw], 0x4000\n\t"
      "s_cmp_eq_u32 %[sw], 0x70000\n\t"
      "s_cselect_b32 %[sw], 0, %[sw]\n\t"
      "buffer_load_dwordx4 v[152:155], %[va0], %[ra], %[sa] offen sc1\n\t"
      "buffer_load_dwordx4 v[156:159], %[va1], %[ra], %[sa] offen sc1\n\t"
      "s_add_u32 %[sa], %[sa], 0x400\n\t"
      "s_cmp_eq_u32 %[sa], 0x7000\n\t"
      "s_cselect_b32 %[sa], 0, %[sa]\n\t"
      "s_waitcnt vmcnt(8)\n\t"
      "ds_write_b128 %[ww], v[168:171] offset:2048\n\t"
      "ds_write_b128 %[ww], v[172:175] offset:3072\n\t"
      "ds_read_b64_tr_b16 v[56:57], %[t0] offset:2048\n\t"
      "ds_read_b64_tr_b16 v[58:59], %[t0] offset:3072\n\t"
      "ds_read_b64_tr_b16 v[60:61], %[t1] offset:2048\n\t"
      "ds_read_b64_tr_b16 v[62:63], %[t1] offset:3072\n\t"
      "buffer_load_dwordx4 v[168:171], %[vw0], %[rw], %[sw] offen\n\t"
      "buffer_load_dwordx4 v[172:175], %[vw1], %[rw], %[sw] offen\n\t"
      "s_add_u32 %[sw], %[sw], 0x4000\n\t"
      "s_cmp_eq_u32 %[sw], 0x70000\n\t"
      "s_cselect_b32 %[sw], 0, %[sw]\n\t"
      "s_waitcnt lgkmcnt(6)\n\t"
      "v_mfma_f32_32x32x16_bf16 v[64:79], v[48:51], v[128:131], 0\n\t"
      "v_mfma_f32_32x32x16_bf16 v[80:95], v[48:51], v[132:135], 0\n\t"
      "v_mfma_f32_32x32x16_bf16 v[96:111], v[52:55], v[128:131], 0\n\t"
      "v_mfma_f32_32x32x16_bf16 v[112:127], v[52:55], v[132:135], 0\n\t"
      "buffer_load_dwordx4 v[128:131], %[va0], %[ra], %[sa] offen sc1\n\t"
      "buffer_load_dwordx4 v[132:135], %[va1], %[ra], %[sa] offen sc1\n\t"
      "s_add_u32 %[sa], %[sa], 0x400\n\t"
      "s_cmp_eq_u32 %[sa], 0x7000\n\t"
      "s_cselect_b32 %[sa], 0, %[sa]\n\t"
      "s_waitcnt vmcnt(10)\n\t"
      "ds_write_b128 %[ww], v[176:179] offset:0\n\t"
      "ds_write_b128 %[ww], v[180:183] offset:1024\n\t"
      "ds_read_b64_tr_b16 v[48:49], %[t0] offset:0\n\t"
      "ds_read_b64_tr_b16 v[50:51], %[t0] offset:1024\n\t"
      "ds_read_b64_tr_b16 v[52:53], %[t1] offset:0\n\t"
      "ds_read_b64_tr_b16 v[54:55], %[t1] offset:1024\n\t"
      "buffer_load_dwordx4 v[176:179], %[vw0], %[rw], %[sw] offen\n\t"
      "buffer_load_dwordx4 v[180:183], %[vw1], %[rw], %[sw] offen\n\t"
      "s_add_u32 %[sw], %[sw], 0x4000\n\t"
      "s_cmp_eq_u32 %[sw], 0x70000\n\t"
      "s_cselect_b32 %[sw], 0, %[sw]\n\t"
      "s_waitcnt lgkmcnt(6)\n\t"
      "v_mfma_f32_32x32x16_bf16 v[64:79], v[56:59], v[136:139], v[64:79]\n\t"
      "v_mfma_f32_32x32x16_bf16 v[80:95], v[56:59], v[140:143], v[80:95]\n\t"
      "v_mfma_f32_32x32x16_bf16 v[96:111], v[60:63], v[136:139], v[96:111]\n\t"
      "v_mfma_f32_32x32x16_bf16 v[112:127], v[60:63], v[140:143], v[112:127]\n\t"
      "buffer_load_dwordx4 v[136:139], %[va0], %[ra], %[sa] offen sc1\n\t"
      "buffer_load_dwordx4 v[140:143], %[va1], %[ra], %[sa] offen sc1\n\t"
      "s_add_u32 %[sa], %[sa], 0x400\n\t"
      "s_cmp_eq_u32 %[sa], 0x7000\n\t"
      "s_cselect_b32 %[sa], 0, %[sa]\n\t"
      "s_waitcnt vmcnt(12)\n\t"
      "ds_write_b128 %[ww], v[184:187] offset:2048\n\t"
      "ds_write_b128 %[ww], v[188:191] offset:3072\n\t"
      "ds_read_b64_tr_b16 v[56:57], %[t0] offset:2048\n\t"
      "ds_read_b64_tr_b16 v[58:59], %[t0] offset:3072\n\t"
      "ds_read_b64_tr_b16 v[60:61], %[t1] offset:2048\n\t"
      "ds_read_b64_tr_b16 v[62:63], %[t1] offset:3072\n\t"
      "buffer_load_dwordx4 v[184:187], %[vw0], %[rw], %[sw] offen\n\t"
      "buffer_load_dwordx4 v[188:191], %[vw1], %[rw], %[sw] offen\n\t"
      "s_add_u32 %[sw], %[sw], 0x4000\n\t"
      "s_cmp_eq_u32 %[sw], 0x70000\n\t"
      "s_cselect_b32 %[sw], 0, %[sw]\n\t"
      "s_waitcnt lgkmcnt(6)\n\t"
      "v_mfma_f32_32x32x16_bf16 v[64:79], v[48:51], v[144:147], v[64:79]\n\t"
      "v_mfma_f32_32x32x16_bf16 v[80:95], v[48:51], v[148:151], v[80:95]\n\t"
      "v_mfma_f32_32x32x16_bf16 v[96:111], v[52:55], v[144:147], v[96:111]\n\t"
      "v_mfma_f32_32x32x16_bf16 v[112:127], v[52:55], v[148:151], v[112:127]\n\t"
      "buffer_load_dwordx4 v[144:147], %[va0], %[ra], %[sa] offen sc1\n\t"
      "buffer_load_dwordx4 v[148:151], %[va1], %[ra], %[sa] offen sc1\n\t"
      "s_add_u32 %[sa], %[sa], 0x400\n\t"
      "s_cmp_eq_u32 %[sa], 0x7000\n\t"
      "s_cselect_b32 %[sa], 0, %[sa]\n\t"
      "1:\n\t"
      "s_waitcnt vmcnt(12)\n\t"
      "ds_write_b128 %[ww], v[160:163] offset:0\n\t"
      "ds_write_b128 %[ww], v[164:167] offset:1024\n\t"
      "ds_read_b64_tr_b16 v[48:49], %[t0] offset:0\n\t"
      "ds_read_b64_tr_b16 v[50:51], %[t0] offset:1024\n\t"
      "ds_read_b64_tr_b16 v[52:53], %[t1] offset:0\n\t"
      "ds_read_b64_tr_b16 v[54:55], %[t1] offset:1024\n\t"
      "buffer_load_dwordx4 v[160:163], %[vw0], %[rw], %[sw] offen\n\t"
      "buffer_load_dwordx4 v[164:167], %[vw1], %[rw], %[sw] offen\n\t"
      "s_add_u32 %[sw], %[sw], 0x4000\n\t"
      "s_cmp_eq_u32 %[sw], 0x70000\n\t"
      "s_cselect_b32 %[sw], 0, %[sw]\n\t"
      "s_waitcnt lgkmcnt(6)\n\t"
      "v_mfma_f32_32x32x16_bf16 v[64:79], v[56:59], v[152:155], v[64:79]\n\t"
      "v_mfma_f32_32x32x16_bf16 v[80:95], v[56:59], v[156:159], v[80:95]\n\t"
      "v_mfma_f32_32x32x16_bf16 v[96:111], v[60:63], v[152:155], v[96:111]\n\t"
      "v_mfma_f32_32x32x16_bf16 v[112:127], v[60:63], v[156:159], v[112:127]\n\t"
      "buffer_load_dwordx4 v[152:155], %[va0], %[ra], %[sa] offen sc1\n\t"
      "buffer_load_dwordx4 v[156:159], %[va1], %[ra], %[sa] offen sc1\n\t"
      "s_add_u32 %[sa], %[sa], 0x400\n\t"
      "s_cmp_eq_u32 %[sa], 0x7000\n\t"
      "s_cselect_b32 %[sa], 0, %[sa]\n\t"
      "s_waitcnt vmcnt(12)\n\t"
      "ds_write_b128 %[ww], v[168:171] offset:2048\n\t"
      "ds_write_b128 %[ww], v[172:175] offset:3072\n\t"
      "ds_read_b64_tr_b16 v[56:57], %[t0] offset:2048\n\t"
      "ds_read_b64_tr_b16 v[58:59], %[t0] offset:3072\n\t"
      "ds_read_b64_tr_b16 v[60:61], %[t1] offset:2048\n\t"
      "ds_read_b64_tr_b16 v[62:63], %[t1] offset:3072\n\t"
      "buffer_load_dwordx4 v[168:171], %[vw0], %[rw], %[sw] offen\n\t"
      "buffer_load_dwordx4 v[172:175], %[vw1], %[rw], %[sw] offen\n\t"
      "s_add_u32 %[sw], %[sw], 0x4000\n\t"
      "s_cmp_eq_u32 %[sw], 0x70000\n\t"
      "s_cselect_b32 %[sw], 0, %[sw]\n\t"
      "s_waitcnt lgkmcnt(6)\n\t"
      "v_mfma_f32_32x32x16_bf16 v[64:79], v[48:51], v[128:131], v[64:79]\n\t"
      "v_mfma_f32_32x32x16_bf16 v[80:95], v[48:51], v[132:135], v[80:95]\n\t"
      "v_mfma_f32_32x32x16_bf16 v[96:111], v[52:55], v[128:131], v[96:111]\n\t"
      "v_mfma_f32_32x32x16_bf16 v[112:127], v[52:55], v[132:135], v[112:127]\n\t"
      "buffer_load_dwordx4 v[128:131], %[va0], %[ra], %[sa] offen sc1\n\t"
      "buffer_load_dwordx4 v[132:135], %[va1], %[ra], %[sa] offen sc1\n\t"
      "s_add_u32 %[sa], %[sa], 0x400\n\t"
      "s_cmp_eq_u32 %[sa], 0x7000\n\t"
      "s_cselect_b32 %[sa], 0, %[sa]\n\t"
      "s_waitcnt vmcnt(12)\n\t"
      "ds_write_b128 %[ww], v[176:179] offset:0\n\t"
      "ds_write_b128 %[ww], v[180:183] offset:1024\n\t"
      "ds_read_b64_tr_b16 v[48:49], %[t0] offset:0\n\t"
      "ds_read_b64_tr_b16 v[50:51], %[t0] offset:1024\n\t"
      "ds_read_b64_tr_b16 v[52:53], %[t1] offset:0\n\t"
      "ds_read_b64_tr_b16 v[54:55], %[t1] offset:1024\n\t"
      "buffer_load_dwordx4 v[176:179], %[vw0], %[rw], %[sw] offen\n\t"
      "buffer_load_dwordx4 v[180:183], %[vw1], %[rw], %[sw] offen\n\t"
      "s_add_u32 %[sw], %[sw], 0x4000\n\t"
      "s_cmp_eq_u32 %[sw], 0x70000\n\t"
      "s_cselect_b32 %[sw], 0, %[sw]\n\t"
      "s_waitcnt lgkmcnt(6)\n\t"
      "v_mfma_f32_32x32x16_bf16 v[64:79], v[56:59], v[136:139], v[64:79]\n\t"
      "v_mfma_f32_32x32x16_bf16 v[80:95], v[56:59], v[140:143], v[80:95]\n\t"
      "v_mfma_f32_32x32x16_bf16 v[96:111], v[60:63], v[136:139], v[96:111]\n\t"
      "v_mfma_f32_32x32x16_bf16 v[112:127], v[60:63], v[140:143], v[112:127]\n\t"
      "buffer_load_dwordx4 v[136:139], %[va0], %[ra], %[sa] offen sc1\n\t"
      "buffer_load_dwordx4 v[140:143], %[va1], %[ra], %[sa] offen sc1\n\t"
      "s_add_u32 %[sa], %[sa], 0x400\n\t"
      "s_cmp_eq_u32 %[sa], 0x7000\n\t"
      "s_cselect_b32 %[sa], 0, %[sa]\n\t"
      "s_waitcnt vmcnt(12)\n\t"
      "ds_write_b128 %[ww], v[184:187] offset:2048\n\t"
      "ds_write_b128 %[ww], v[188:191] offset:3072\n\t"
      "ds_read_b64_tr_b16 v[56:57], %[t0] offset:2048\n\t"
      "ds_read_b64_tr_b16 v[58:59], %[t0] offset:3072\n\t"
      "ds_read_b64_tr_b16 v[60:61], %[t1] offset:2048\n\t"
      "ds_read_b64_tr_b16 v[62:63], %[t1] offset:3072\n\t"
      "buffer_load_dwordx4 v[184:187], %[vw0], %[rw], %[sw] offen\n\t"
      "buffer_load_dwordx4 v[188:191], %[vw1], %[rw], %[sw] offen\n\t"
      "s_add_u32 %[sw], %[sw], 0x4000\n\t"
      "s_cmp_eq_u32 %[sw], 0x70000\n\t"
      "s_cselect_b32 %[sw], 0, %[sw]\n\t"
      "s_waitcnt lgkmcnt(6)\n\t"
      "v_mfma_f32_32x32x16_bf16 v[64:79], v[48:51], v[144:147], v[64:79]\n\t"
      "v_mfma_f32_32x32x16_bf16 v[80:95], v[48:51], v[148:151], v[80:95]\n\t"
      "v_mfma_f32_32x32x16_bf16 v[96:111], v[52:55], v[144:147], v[96:111]\n\t"
      "v_mfma_f32_32x32x16_bf16 v[112:127], v[52:55], v[148:151], v[112:127]\n\t"
      "buffer_load_dwordx4 v[144:147], %[va0], %[ra], %[sa] offen sc1\n\t"
      "buffer_load_dwordx4 v[148:151], %[va1], %[ra], %[sa] offen sc1\n\t"
      "s_add_u32 %[sa], %[sa], 0x400\n\t"
      "s_cmp_eq_u32 %[sa], 0x7000\n\t"
      "s_cselect_b32 %[sa], 0, %[sa]\n\t"
      "s_sub_u32 %[cnt], %[cnt], 1\n\t"
      "s_cmp_lg_u32 %[cnt], 0\n\t"
      "s_cbranch_scc1 1b\n\t"
      "s_waitcnt vmcnt(12)\n\t"
      "s_waitcnt lgkmcnt(0)\n\t"
      "v_mfma_f32_32x32x16_bf16 v[64:79], v[56:59], v[152:155], v[64:79]\n\t"
      "v_mfma_f32_32x32x16_bf16 v[80:95], v[56:59], v[156:159], v[80:95]\n\t"
      "v_mfma_f32_32x32x16_bf16 v[96:111], v[60:63], v[152:155], v[96:111]\n\t"
      "v_mfma_f32_32x32x16_bf16 v[112:127], v[60:63], v[156:159], v[112:127]\n\t"
      "buffer_load_dwordx4 v[152:155], %[va0], %[ra], %[sa] offen sc1\n\t"
      "buffer_load_dwordx4 v[156:159], %[va1], %[ra], %[sa] offen sc1\n\t"
      "s_add_u32 %[sa], %[sa], 0x400\n\t"
      "s_cmp_eq_u32 %[sa], 0x7000\n\t"
      "s_cselect_b32 %[sa], 0, %[sa]\n\t"
      "buffer_load_dwordx4 v[224:227], %[vw0], %[rw], %[sw] offen\n\t"
      "buffer_load_dwordx4 v[228:231], %[vw1], %[rw], %[sw] offen\n\t"
      "s_add_u32 %[sw], %[sw], 0x4000\n\t"
      "s_cmp_eq_u32 %[sw], 0x70000\n\t"
      "s_cselect_b32 %[sw], 0, %[sw]\n\t"
      "buffer_load_dwordx4 v[192:195], %[va0], %[ra], %[sa] offen sc1\n\t"
      "buffer_load_dwordx4 v[196:199], %[va1], %[ra], %[sa] offen sc1\n\t"
      "s_add_u32 %[sa], %[sa], 0x400\n\t"
      "s_cmp_eq_u32 %[sa], 0x7000\n\t"
      "s_cselect_b32 %[sa], 0, %[sa]\n\t"
      "buffer_load_dwordx4 v[232:235], %[vw0], %[rw], %[sw] offen\n\t"
      "buffer_load_dwordx4 v[236:239], %[vw1], %[rw], %[sw] offen\n\t"
      "s_add_u32 %[sw], %[sw], 0x4000\n\t"
      "s_cmp_eq_u32 %[sw], 0x70000\n\t"
      "s_cselect_b32 %[sw], 0, %[sw]\n\t"
      "buffer_load_dwordx4 v[200:203], %[va0], %[ra], %[sa] offen sc1\n\t"
      "buffer_load_dwordx4 v[204:207], %[va1], %[ra], %[sa] offen sc1\n\t"
      "s_add_u32 %[sa], %[sa], 0x400\n\t"
      "s_cmp_eq_u32 %[sa], 0x7000\n\t"
      "s_cselect_b32 %[sa], 0, %[sa]\n\t"
      "buffer_load_dwordx4 v[240:243], %[vw0], %[rw], %[sw] offen\n\t"
      "buffer_load_dwordx4 v[244:247], %[vw1], %[rw], %[sw] offen\n\t"
      "s_add_u32 %[sw], %[sw], 0x4000\n\t"
      "s_cmp_eq_u32 %[sw], 0x70000\n\t"
      "s_cselect_b32 %[sw], 0, %[sw]\n\t"
      "buffer_load_dwordx4 v[208:211], %[va0], %[ra], %[sa] offen sc1\n\t"
      "buffer_load_dwordx4 v[212:215], %[va1], %[ra], %[sa] offen sc1\n\t"
      "s_add_u32 %[sa], %[sa], 0x400\n\t"
      "s_cmp_eq_u32 %[sa], 0x7000\n\t"
      "s_cselect_b32 %[sa], 0, %[sa]\n\t"
      "buffer_load_dwordx4 v[248:251], %[vw0], %[rw], %[sw] offen\n\t"
      "buffer_load_dwordx4 v[252:255], %[vw1], %[rw], %[sw] offen\n\t"
      "s_add_u32 %[sw], %[sw], 0x4000\n\t"
      "s_cmp_eq_u32 %[sw], 0x70000\n\t"
      "s_cselect_b32 %[sw], 0, %[sw]\n\t"
      "buffer_load_dwordx4 v[216:219], %[va0], %[ra], %[sa] offen sc1\n\t"
      "buffer_load_dwordx4 v[220:223], %[va1], %[ra], %[sa] offen sc1\n\t"
      "s_add_u32 %[sa], %[sa], 0x400\n\t"
      "s_cmp_eq_u32 %[sa], 0x7000\n\t"
      "s_cselect_b32 %[sa], 0, %[sa]\n\t"
      "s_setprio 1\n\t"
      "42:\n\t"
      "ds_read_b32 v46, %[cb] offset:32\n\t"
      "s_sleep 8\n\t"
      "s_waitcnt lgkmcnt(0)\n\t"
      "v_readfirstlane_b32 %[tmp], v46\n\t"
      "s_nop 3\n\t"
      "s_cmp_lt_u32 %[tmp], 4\n\t"
      "s_cbranch_scc1 42b\n\t"
      "s_add_u32 m0, %[mo], 0\n\t"
      "s_add_u32 %[tmp], %[so], 0\n\t"
      "buffer_load_dwordx4 %[vt], %[ro], %[tmp] offen lds\n\t"
      "s_add_u32 m0, %[mo], 1024\n\t"
      "s_add_u32 %[tmp], %[so], 8192\n\t"
      "buffer_load_dwordx4 %[vt], %[ro], %[tmp] offen lds\n\t"
      "s_add_u32 m0, %[mo], 2048\n\t"
      "s_add_u32 %[tmp], %[so], 16384\n\t"
      "buffer_load_dwordx4 %[vt], %[ro], %[tmp] offen lds\n\t"
      "s_add_u32 m0, %[mo], 3072\n\t"
      "s_add_u32 %[tmp], %[so], 24576\n\t"
      "buffer_load_dwordx4 %[vt], %[ro], %[tmp] offen lds\n\t"
      "s_add_u32 m0, %[mo], 4096\n\t"
      "s_add_u32 %[tmp], %[so], 32768\n\t"
      "buffer_load_dwordx4 %[vt], %[ro], %[tmp] offen lds\n\t"
      "s_add_u32 m0, %[mo], 5120\n\t"
      "s_add_u32 %[tmp], %[so], 40960\n\t"
      "buffer_load_dwordx4 %[vt], %[ro], %[tmp] offen lds\n\t"
      "s_add_u32 m0, %[mo], 6144\n\t"
      "s_add_u32 %[tmp], %[so], 49152\n\t"
      "buffer_load_dwordx4 %[vt], %[ro], %[tmp] offen lds\n\t"
      "s_add_u32 m0, %[mo], 7168\n\t"
      "s_add_u32 %[tmp], %[so], 57344\n\t"
      "buffer_load_dwordx4 %[vt], %[ro], %[tmp] offen lds\n\t"
      "s_add_u32 m0, %[mo], 8192\n\t"
      "s_add_u32 %[tmp], %[so], 65536\n\t"
      "buffer_load_dwordx4 %[vt], %[ro], %[tmp] offen lds\n\t"
      "s_add_u32 m0, %[mo], 9216\n\t"
      "s_add_u32 %[tmp], %[so], 73728\n\t"
      "buffer_load_dwordx4 %[vt], %[ro], %[tmp] offen lds\n\t"
      "s_add_u32 m0, %[mo], 10240\n\t"
      "s_add_u32 %[tmp], %[so], 81920\n\t"
      "buffer_load_dwordx4 %[vt], %[ro], %[tmp] offen lds\n\t"
      "s_add_u32 m0, %[mo], 11264\n\t"
      "s_add_u32 %[tmp], %[so], 90112\n\t"
      "buffer_load_dwordx4 %[vt], %[ro], %[tmp] offen lds\n\t"
      "s_add_u32 m0, %[mo], 12288\n\t"
      "s_add_u32 %[tmp], %[so], 98304\n\t"
      "buffer_load_dwordx4 %[vt], %[ro], %[tmp] offen lds\n\t"
      "s_add_u32 m0, %[mo], 13312\n\t"
      "s_add_u32 %[tmp], %[so], 106496\n\t"
      "buffer_load_dwordx4 %[vt], %[ro], %[tmp] offen lds\n\t"
      "s_add_u32 m0, %[mo], 14336\n\t"
      "s_add_u32 %[tmp], %[so], 114688\n\t"
      "buffer_load_dwordx4 %[vt], %[ro], %[tmp] offen lds\n\t"
      "s_add_u32 m0, %[mo], 15360\n\t"
      "s_add_u32 %[tmp], %[so], 122880\n\t"
      "buffer_load_dwordx4 %[vt], %[ro], %[tmp] offen lds\n\t"
      "s_waitcnt vmcnt(0)\n\t"
      "s_mov_b64 exec, 1\n\t"
      "ds_add_u32 %[cb], v43 offset:36\n\t"
      "s_mov_b64 exec, -1\n\t"
      "39:\n\t"
      "ds_read_b32 v46, %[cb] offset:24\n\t"
      "s_sleep 4\n\t"
      "s_waitcnt lgkmcnt(0)\n\t"
      "v_readfirstlane_b32 %[tmp], v46\n\t"
      "s_nop 3\n\t"
      "s_cmp_lt_u32 %[tmp], 4\n\t"
      "s_cbranch_scc1 39b\n\t"
      "s_setprio 1\n\t"
      "s_waitcnt vmcnt(0)\n\t"
      "ds_write_b128 %[ww], v[160:163] offset:0\n\t"
      "ds_write_b128 %[ww], v[164:167] offset:1024\n\t"
      "ds_read_b64_tr_b16 v[48:49], %[t0] offset:0\n\t"
      "ds_read_b64_tr_b16 v[50:51], %[t0] offset:1024\n\t"
      "ds_read_b64_tr_b16 v[52:53], %[t1] offset:0\n\t"
      "ds_read_b64_tr_b16 v[54:55], %[t1] offset:1024\n\t"
      "v_sub_f32 v44, 1.0, v44\n\t"
      "v_sub_f32 v45, 1.0, v45\n\t"
      "ds_write_b128 %[ww], v[168:171] offset:2048\n\t"
      "ds_write_b128 %[ww], v[172:175] offset:3072\n\t"
      "ds_read_b64_tr_b16 v[56:57], %[t0] offset:2048\n\t"
      "ds_read_b64_tr_b16 v[58:59], %[t0] offset:3072\n\t"
      "ds_read_b64_tr_b16 v[60:61], %[t1] offset:2048\n\t"
      "ds_read_b64_tr_b16 v[62:63], %[t1] offset:3072\n\t"
      "s_waitcnt lgkmcnt(6)\n\t"
      "v_mfma_f32_32x32x16_bf16 v[64:79], v[48:51], v[128:131], v[64:79]\n\t"
      "v_mfma_f32_32x32x16_bf16 v[80:95], v[48:51], v[132:135], v[80:95]\n\t"
      "v_mfma_f32_32x32x16_bf16 v[96:111], v[52:55], v[128:131], v[96:111]\n\t"
      "v_mfma_f32_32x32x16_bf16 v[112:127], v[52:55], v[132:135], v[112:127]\n\t"
      "ds_write_b128 %[ww], v[176:179] offset:0\n\t"
      "ds_write_b128 %[ww], v[180:183] offset:1024\n\t"
      "ds_read_b64_tr_b16 v[48:49], %[t0] offset:0\n\t"
      "ds_read_b64_tr_b16 v[50:51], %[t0] offset:1024\n\t"
      "ds_read_b64_tr_b16 v[52:53], %[t1] offset:0\n\t"
      "ds_read_b64_tr_b16 v[54:55], %[t1] offset:1024\n\t"
      "s_waitcnt lgkmcnt(6)\n\t"
      "v_mfma_f32_32x32x16_bf16 v[64:79], v[56:59], v[136:139], v[64:79]\n\t"
      "v_mfma_f32_32x32x16_bf16 v[80:95], v[56:59], v[140:143], v[80:95]\n\t"
      "v_mfma_f32_32x32x16_bf16 v[96:111], v[60:63], v[136:139], v[96:111]\n\t"
      "v_mfma_f32_32x32x16_bf16 v[112:127], v[60:63], v[140:143], v[112:127]\n\t"
      "ds_write_b128 %[ww], v[184:187] offset:2048\n\t"
      "ds_write_b128 %[ww], v[188:191] offset:3072\n\t"
      "ds_read_b64_tr_b16 v[56:57], %[t0] offset:2048\n\t"
      "ds_read_b64_tr_b16 v[58:59], %[t0] offset:3072\n\t"
      "ds_read_b64_tr_b16 v[60:61], %[t1] offset:2048\n\t"
      "ds_read_b64_tr_b16 v[62:63], %[t1] offset:3072\n\t"
      "s_waitcnt lgkmcnt(6)\n\t"
      "v_mfma_f32_32x32x16_bf16 v[64:79], v[48:51], v[144:147], v[64:79]\n\t"
      "v_mfma_f32_32x32x16_bf16 v[80:95], v[48:51], v[148:151], v[80:95]\n\t"
      "v_mfma_f32_32x32x16_bf16 v[96:111], v[52:55], v[144:147], v[96:111]\n\t"
      "v_mfma_f32_32x32x16_bf16 v[112:127], v[52:55], v[148:151], v[112:127]\n\t"
      "ds_write_b128 %[ww], v[224:227] offset:0\n\t"
      "ds_write_b128 %[ww], v[228:231] offset:1024\n\t"
      "ds_read_b64_tr_b16 v[48:49], %[t0] offset:0\n\t"
      "ds_read_b64_tr_b16 v[50:51], %[t0] offset:1024\n\t"
      "ds_read_b64_tr_b16 v[52:53], %[t1] offset:0\n\t"
      "ds_read_b64_tr_b16 v[54:55], %[t1] offset:1024\n\t"
      "s_waitcnt lgkmcnt(6)\n\t"
      "v_mfma_f32_32x32x16_bf16 v[64:79], v[56:59], v[152:155], v[64:79]\n\t"
      "v_mfma_f32_32x32x16_bf16 v[80:95], v[56:59], v[156:159], v[80:95]\n\t"
      "v_mfma_f32_32x32x16_bf16 v[96:111], v[60:63], v[152:155], v[96:111]\n\t"
      "v_mfma_f32_32x32x16_bf16 v[112:127], v[60:63], v[156:159], v[112:127]\n\t"
      "ds_write_b128 %[ww], v[232:235] offset:2048\n\t"
      "ds_write_b128 %[ww], v[236:239] offset:3072\n\t"
      "ds_read_b64_tr_b16 v[56:57], %[t0] offset:2048\n\t"
      "ds_read_b64_tr_b16 v[58:59], %[t0] offset:3072\n\t"
      "ds_read_b64_tr_b16 v[60:61], %[t1] offset:2048\n\t"
      "ds_read_b64_tr_b16 v[62:63], %[t1] offset:3072\n\t"
      "s_waitcnt lgkmcnt(6)\n\t"
      "v_mfma_f32_32x32x16_bf16 v[64:79], v[48:51], v[192:195], v[64:79]\n\t"
      "v_mfma_f32_32x32x16_bf16 v[80:95], v[48:51], v[196:199], v[80:95]\n\t"
      "v_mfma_f32_32x32x16_bf16 v[96:111], v[52:55], v[192:195], v[96:111]\n\t"
      "v_mfma_f32_32x32x16_bf16 v[112:127], v[52:55], v[196:199], v[112:127]\n\t"
      "ds_write_b128 %[ww], v[240:243] offset:0\n\t"
      "ds_write_b128 %[ww], v[244:247] offset:1024\n\t"
      "ds_read_b64_tr_b16 v[48:49], %[t0] offset:0\n\t"
      "ds_read_b64_tr_b16 v[50:51], %[t0] offset:1024\n\t"
      "ds_read_b64_tr_b16 v[52:53], %[t1] offset:0\n\t"
      "ds_read_b64_tr_b16 v[54:55], %[t1] offset:1024\n\t"
      "s_waitcnt lgkmcnt(6)\n\t"
      "v_mfma_f32_32x32x16_bf16 v[64:79], v[56:59], v[200:203], v[64:79]\n\t"
      "v_mfma_f32_32x32x16_bf16 v[80:95], v[56:59], v[204:207], v[80:95]\n\t"
      "v_mfma_f32_32x32x16_bf16 v[96:111], v[60:63], v[200:203], v[96:111]\n\t"
      "v_mfma_f32_32x32x16_bf16 v[112:127], v[60:63], v[204:207], v[112:127]\n\t"
      "ds_write_b128 %[ww], v[248:251] offset:2048\n\t"
      "ds_write_b128 %[ww], v[252:255] offset:3072\n\t"
      "ds_read_b64_tr_b16 v[56:57], %[t0] offset:2048\n\t"
      "ds_read_b64_tr_b16 v[58:59], %[t0] offset:3072\n\t"
      "ds_read_b64_tr_b16 v[60:61], %[t1] offset:2048\n\t"
      "ds_read_b64_tr_b16 v[62:63], %[t1] offset:3072\n\t"
      "s_waitcnt lgkmcnt(6)\n\t"
      "v_mfma_f32_32x32x16_bf16 v[64:79], v[48:51], v[208:211], v[64:79]\n\t"
      "v_mfma_f32_32x32x16_bf16 v[80:95], v[48:51], v[212:215], v[80:95]\n\t"
      "v_mfma_f32_32x32x16_bf16 v[96:111], v[52:55], v[208:211], v[96:111]\n\t"
      "v_mfma_f32_32x32x16_bf16 v[112:127], v[52:55], v[212:215], v[112:127]\n\t"
      "s_waitcnt lgkmcnt(0)\n\t"
      "v_mfma_f32_32x32x16_bf16 v[64:79], v[56:59], v[216:219], v[64:79]\n\t"
      "v_mfma_f32_32x32x16_bf16 v[80:95], v[56:59], v[220:223], v[80:95]\n\t"
      "v_mfma_f32_32x32x16_bf16 v[96:111], v[60:63], v[216:219], v[96:111]\n\t"
      "v_mfma_f32_32x32x16_bf16 v[112:127], v[60:63], v[220:223], v[112:127]\n\t"
      "s_nop 9\n\t"
      "v_mul_f32 v64, v44, v64\n\t"
      "v_mul_f32 v65, v44, v65\n\t"
      "v_mul_f32 v66, v44, v66\n\t"
      "v_mul_f32 v67, v44, v67\n\t"
      "v_mul_f32 v68, v44, v68\n\t"
      "v_mul_f32 v69, v44, v69\n\t"
      "v_mul_f32 v70, v44, v70\n\t"
      "v_mul_f32 v71, v44, v71\n\t"
      "v_mul_f32 v72, v44, v72\n\t"
      "v_mul_f32 v73, v44, v73\n\t"
      "v_mul_f32 v74, v44, v74\n\t"
      "v_mul_f32 v75, v44, v75\n\t"
      "v_mul_f32 v76, v44, v76\n\t"
      "v_mul_f32 v77, v44, v77\n\t"
      "v_mul_f32 v78, v44, v78\n\t"
      "v_mul_f32 v79, v44, v79\n\t"
      "v_mul_f32 v80, v45, v80\n\t"
      "v_mul_f32 v81, v45, v81\n\t"
      "v_mul_f32 v82, v45, v82\n\t"
      "v_mul_f32 v83, v45, v83\n\t"
      "v_mul_f32 v84, v45, v84\n\t"
      "v_mul_f32 v85, v45, v85\n\t"
      "v_mul_f32 v86, v45, v86\n\t"
      "v_mul_f32 v87, v45, v87\n\t"
      "v_mul_f32 v88, v45, v88\n\t"
      "v_mul_f32 v89, v45, v89\n\t"
      "v_mul_f32 v90, v45, v90\n\t"
      "v_mul_f32 v91, v45, v91\n\t"
      "v_mul_f32 v92, v45, v92\n\t"
      "v_mul_f32 v93, v45, v93\n\t"
      "v_mul_f32 v94, v45, v94\n\t"
      "v_mul_f32 v95, v45, v95\n\t"
      "v_mul_f32 v96, v44, v96\n\t"
      "v_mul_f32 v97, v44, v97\n\t"
      "v_mul_f32 v98, v44, v98\n\t"
      "v_mul_f32 v99, v44, v99\n\t"
      "v_mul_f32 v100, v44, v100\n\t"
      "v_mul_f32 v101, v44, v101\n\t"
      "v_mul_f32 v102, v44, v102\n\t"
      "v_mul_f32 v103, v44, v103\n\t"
      "v_mul_f32 v104, v44, v104\n\t"
      "v_mul_f32 v105, v44, v105\n\t"
      "v_mul_f32 v106, v44, v106\n\t"
      "v_mul_f32 v107, v44, v107\n\t"
      "v_mul_f32 v108, v44, v108\n\t"
      "v_mul_f32 v109, v44, v109\n\t"
      "v_mul_f32 v110, v44, v110\n\t"
      "v_mul_f32 v111, v44, v111\n\t"
      "v_mul_f32 v112, v45, v112\n\t"
      "v_mul_f32 v113, v45, v113\n\t"
      "v_mul_f32 v114, v45, v114\n\t"
      "v_mul_f32 v115, v45, v115\n\t"
      "v_mul_f32 v116, v45, v116\n\t"
      "v_mul_f32 v117, v45, v117\n\t"
      "v_mul_f32 v118, v45, v118\n\t"
      "v_mul_f32 v119, v45, v119\n\t"
      "v_mul_f32 v120, v45, v120\n\t"
      "v_mul_f32 v121, v45, v121\n\t"
      "v_mul_f32 v122, v45, v122\n\t"
      "v_mul_f32 v123, v45, v123\n\t"
      "v_mul_f32 v124, v45, v124\n\t"
      "v_mul_f32 v125, v45, v125\n\t"
      "v_mul_f32 v126, v45, v126\n\t"
      "v_mul_f32 v127, v45, v127\n\t"
      "ds_write_b128 %[rwr], v[64:67] offset:0\n\t"
      "ds_write_b128 %[rwr], v[68:71] offset:32\n\t"
      "ds_write_b128 %[rwr], v[72:75] offset:64\n\t"
      "ds_write_b128 %[rwr], v[76:79] offset:96\n\t"
      "ds_write_b128 %[rwr], v[80:83] offset:8704\n\t"
      "ds_write_b128 %[rwr], v[84:87] offset:8736\n\t"
      "ds_write_b128 %[rwr], v[88:91] offset:8768\n\t"
      "ds_write_b128 %[rwr], v[92:95] offset:8800\n\t"
      "ds_write_b128 %[rwr], v[96:99] offset:128\n\t"
      "ds_write_b128 %[rwr], v[100:103] offset:160\n\t"
      "ds_write_b128 %[rwr], v[104:107] offset:192\n\t"
      "ds_write_b128 %[rwr], v[108:111] offset:224\n\t"
      "ds_write_b128 %[rwr], v[112:115] offset:8832\n\t"
      "ds_write_b128 %[rwr], v[116:119] offset:8864\n\t"
      "ds_write_b128 %[rwr], v[120:123] offset:8896\n\t"
      "ds_write_b128 %[rwr], v[124:127] offset:8928\n\t"
      "v_add_u32 v47, 69632, %[rrd]\n\t"
      "s_waitcnt lgkmcnt(0)\n\t"
      "s_mov_b64 exec, 1\n\t"
      "ds_add_u32 %[cb], v43 offset:28\n\t"
      "s_mov_b64 exec, -1\n\t"
      "37:\n\t"
      "ds_read_b32 v46, %[cb] offset:28\n\t"
      "s_waitcnt lgkmcnt(0)\n\t"
      "v_readfirstlane_b32 %[tmp], v46\n\t"
      "s_nop 3\n\t"
      "s_cmp_lt_u32 %[tmp], 8\n\t"
      "s_cbranch_scc1 37b\n\t"
      "ds_read_b128 v[128:131], %[rrd] offset:0\n\t"
      "ds_read_b128 v[132:135], %[rrd] offset:16\n\t"
      "ds_read_b128 v[136:139], %[rrd] offset:17408\n\t"
      "ds_read_b128 v[140:143], %[rrd] offset:17424\n\t"
      "ds_read_b128 v[144:147], %[rrd] offset:34816\n\t"
      "ds_read_b128 v[148:151], %[rrd] offset:34832\n\t"
      "ds_read_b128 v[152:155], %[rrd] offset:52224\n\t"
      "ds_read_b128 v[156:159], %[rrd] offset:52240\n\t"
      "ds_read_b128 v[160:163], v47 offset:0\n\t"
      "ds_read_b128 v[164:167], v47 offset:16\n\t"
      "ds_read_b128 v[168:171], v47 offset:17408\n\t"
      "ds_read_b128 v[172:175], v47 offset:17424\n\t"
      "ds_read_b128 v[176:179], v47 offset:34816\n\t"
      "ds_read_b128 v[180:183], v47 offset:34832\n\t"
      "ds_read_b128 v[184:187], v47 offset:52224\n\t"
      "ds_read_b128 v[188:191], v47 offset:52240\n\t"
      "s_waitcnt lgkmcnt(12)\n\t"
      "v_pk_add_f32 v[128:129], v[128:129], v[136:137]\n\t"
      "v_pk_add_f32 v[130:131], v[130:131], v[138:139]\n\t"
      "v_pk_add_f32 v[132:133], v[132:133], v[140:141]\n\t"
      "v_pk_add_f32 v[134:135], v[134:135], v[142:143]\n\t"
      "s_waitcnt lgkmcnt(8)\n\t"
      "v_pk_add_f32 v[144:145], v[144:145], v[152:153]\n\t"
      "v_pk_add_f32 v[146:147], v[146:147], v[154:155]\n\t"
      "v_pk_add_f32 v[148:149], v[148:149], v[156:157]\n\t"
      "v_pk_add_f32 v[150:151], v[150:151], v[158:159]\n\t"
      "v_pk_add_f32 v[128:129], v[128:129], v[144:145]\n\t"
      "v_pk_add_f32 v[130:131], v[130:131], v[146:147]\n\t"
      "v_pk_add_f32 v[132:133], v[132:133], v[148:149]\n\t"
      "v_pk_add_f32 v[134:135], v[134:135], v[150:151]\n\t"
      "s_waitcnt lgkmcnt(4)\n\t"
      "v_pk_add_f32 v[160:161], v[160:161], v[168:169]\n\t"
      "v_pk_add_f32 v[162:163], v[162:163], v[170:171]\n\t"
      "v_pk_add_f32 v[164:165], v[164:165], v[172:173]\n\t"
      "v_pk_add_f32 v[166:167], v[166:167], v[174:175]\n\t"
      "v_pk_add_f32 v[128:129], v[128:129], v[160:161]\n\t"
      "v_pk_add_f32 v[130:131], v[130:131], v[162:163]\n\t"
      "v_pk_add_f32 v[132:133], v[132:133], v[164:165]\n\t"
      "v_pk_add_f32 v[134:135], v[134:135], v[166:167]\n\t"
      "s_waitcnt lgkmcnt(0)\n\t"
      "v_pk_add_f32 v[176:177], v[176:177], v[184:185]\n\t"
      "v_pk_add_f32 v[178:179], v[178:179], v[186:187]\n\t"
      "v_pk_add_f32 v[180:181], v[180:181], v[188:189]\n\t"
      "v_pk_add_f32 v[182:183], v[182:183], v[190:191]\n\t"
      "v_pk_add_f32 v[128:129], v[128:129], v[176:177]\n\t"
      "v_pk_add_f32 v[130:131], v[130:131], v[178:179]\n\t"
      "v_pk_add_f32 v[132:133], v[132:133], v[180:181]\n\t"
      "v_pk_add_f32 v[134:135], v[134:135], v[182:183]\n\t"
      "v_cvt_pk_bf16_f32 v136, v128, v129\n\t"
      "v_cvt_pk_bf16_f32 v137, v130, v131\n\t"
      "v_cvt_pk_bf16_f32 v138, v132, v133\n\t"
      "v_cvt_pk_bf16_f32 v139, v134, v135\n\t"
      "buffer_store_dwordx4 v[136:139], %[vo], %[rout], 0 offen sc1\n\t"
      "s_nop 1\n\t"
      : [sw] "+s"(sw), [sa] "+s"(sa), [cnt] "+s"(cnt), [cap] "+s"(cap), [tmp] "=&s"(tmp)
      : OUT_OPERANDS(lds0 + L_Q - 8192 + f * 4096), [va0] "v"(va0), [va1] "v"(va0 + 112 * 1024),
        [fo] "v"(F_2 + (((lane & 31) < 30 ? (lane & 31) >> 1 : 0) * 64 + 2 * tb + (lane & 1)) * FS), [vg] "v"(WS_G + (tb * 64 + r) * 4),
        [vt] "v"((lane >> 3) * 1024 + (((lane & 7) ^ ((lane >> 2) & 4)) << 4)), [so] "s"(f * 131072 + nb * 128), [mo] "s"(lds0 + f * 64 * RED_STRIDE),
        [rw] "s"(__builtin_amdgcn_make_buffer_rsrc((void*)(w2 + w2o), 0, DFF * 1024 - w2o, 0x00020000))
      : "v43", "v44", "v45", "v46", "v47", "v48", "v49", "v50", "v51", "v52", "v53", "v54", "v55", "v56", "v57", "v58", "v59", "v60", "v61", "v62", "v63", "v64", "v65", "v66", "v67", "v68", "v69", "v70", "v71", "v72", "v73", "v74", "v75", "v76", "v77", "v78", "v79", "v80", "v81", "v82", "v83", "v84", "v85", "v86", "v87", "v88", "v89", "v90", "v91", "v92", "v93", "v94", "v95", "v96", "v97", "v98", "v99", "v100", "v101", "v102", "v103", "v104", "v105", "v106", "v107", "v108", "v109", "v110", "v111", "v112", "v113", "v114", "v115", "v116", "v117", "v118", "v119", "v120", "v121", "v122", "v123", "v124", "v125", "v126", "v127", "v128", "v129", "v130", "v131", "v132", "v133", "v134", "v135", "v136", "v137", "v138", "v139", "v140", "v141", "v142", "v143", "v144", "v145", "v146", "v147", "v148", "v149", "v150", "v151", "v152", "v153", "v154", "v155", "v156", "v157", "v158", "v159", "v160", "v161", "v162", "v163", "v164", "v165", "v166", "v167", "v168", "v169", "v170", "v171", "v172", "v173", "v174", "v175", "v176", "v177", "v178", "v179", "v180", "v181", "v182", "v183", "v184", "v185", "v186", "v187", "v188", "v189", "v190", "v191", "v192", "v193", "v194", "v195", "v196", "v197", "v198", "v199", "v200", "v201", "v202", "v203", "v204", "v205", "v206", "v207", "v208", "v209", "v210", "v211", "v212", "v213", "v214", "v215", "v216", "v217", "v218", "v219", "v220", "v221", "v222", "v223", "v224", "v225", "v226", "v227", "v228", "v229", "v230", "v231", "v232", "v233", "v234", "v235", "v236", "v237", "v238", "v239", "v240", "v241", "v242", "v243", "v244", "v245", "v246", "v247", "v248", "v249", "v250", "v251", "v252", "v253", "v254", "v255", "scc", "vcc", "memory");
  }
}

extern "C" void kernel_launch(void* const* d_in, const int* in_sizes, int n_in, void* d_out, int out_size, void* d_ws, size_t ws_size, hipStream_t stream) {
  (void)hipFuncSetAttribute((const void*)fused, hipFuncAttributeMaxDynamicSharedMemorySize, LDS_BYTES);
  hipLaunchKernelGGL(fused, dim3(256), dim3(512), LDS_BYTES, stream, (const char*)d_in[0], (const char*)d_in[1], (const char*)d_in[2], (const char*)d_in[4],
                     (const char*)d_in[5], (const char*)d_in[6], (char*)d_ws, (char*)d_out);
}
```
